```python
import torch

def _softmax(x, axis=-1):
    return torch.softmax(x, dim=axis)

def _gelu(x):
    return torch.nn.functional.gelu(x, approximate='tanh')

def _silu(x):
    return torch.nn.functional.silu(x)

def _sigmoid(x):
    return torch.sigmoid(x)

def _layernorm(x, axis=-1, eps=1e-05):
    m = x.mean(dim=axis, keepdim=True)
    v = x.var(dim=axis, keepdim=True, unbiased=False)
    return (x - m) / torch.sqrt(v + eps)

def _scatter_add(tgt, idx, src, axis=0):
    return tgt.clone().index_add_(axis, idx, src)

def _segment_sum(src, seg, n):
    out = torch.zeros((n,) + src.shape[1:], dtype=src.dtype)
    return out.index_add_(0, seg, src)

def _pad(x, pw):
    flat = tuple((v for pair in reversed(pw) for v in pair))
    return torch.nn.functional.pad(x, flat)
kernel_name = 'parallel_sum__compressed_kv_cla__mixer_gmlp__prompt'

def setup_inputs():

    _w = lambda a: (lambda t: t.to(torch.bfloat16) if t.is_floating_point() else t)(torch.as_tensor(a))

    def _A_setup():
        B, H, Sq, Sk, D, M = (1, 32, 2048, 2048, 128, 512)
        Dc = 4 * D
        return {'x': _w((lambda *s: torch.randn(*s, dtype=torch.bfloat16) * 0.05)(B, Sq, M)), 'c_kv': _w((lambda *s: torch.randn(*s, dtype=torch.bfloat16) * 1.6)(B, Sk, Dc)), 'w_q': _w((lambda *s: torch.randn(*s, dtype=torch.bfloat16) * 1.6)(M, H, D) * M ** (-0.5)), 'w_uk': _w((lambda *s: torch.randn(*s, dtype=torch.bfloat16) * 1.6)(Dc, H, D) * Dc ** (-0.5)), 'w_uv': _w((lambda *s: torch.randn(*s, dtype=torch.bfloat16) * 1.6)(Dc, H, D) * Dc ** (-0.5)), 'w_o': _w((lambda *s: torch.randn(*s, dtype=torch.bfloat16) * 1.6)(H, D, M) * (H * D) ** (-0.5))}
    _A_in = _A_setup()

    def _B_setup():
        B, S, D, F = (1, 2048, 512, 1376)
        Fh = F // 2
        return {'x': _w((lambda *s: torch.randn(*s, dtype=torch.bfloat16) * 0.05)(B, S, D)), 'w_in': _w((lambda *s: torch.randn(*s, dtype=torch.bfloat16) * 0.4)(D, 2 * Fh)), 'w_sp': _w(0.01 * (lambda *s: torch.randn(*s, dtype=torch.bfloat16) * 0.4)(S, S)), 'b_sp': _w(1.0 + 0.01 * (lambda *s: torch.randn(*s, dtype=torch.bfloat16) * 0.05)(S)), 'w_out': _w((lambda *s: torch.randn(*s, dtype=torch.bfloat16) * 0.05)(Fh, D))}
    _B_in = _B_setup()
    return {'A_x': _A_in['x'], 'A_c_kv': _A_in['c_kv'], 'A_w_q': _A_in['w_q'], 'A_w_uk': _A_in['w_uk'], 'A_w_uv': _A_in['w_uv'], 'A_w_o': _A_in['w_o'], 'B_x': _B_in['x'], 'B_w_in': _B_in['w_in'], 'B_w_sp': _B_in['w_sp'], 'B_b_sp': _B_in['b_sp'], 'B_w_out': _B_in['w_out']}

def reference(A_x, A_c_kv, A_w_q, A_w_uk, A_w_uv, A_w_o, B_x, B_w_in, B_w_sp, B_b_sp, B_w_out):

    def _A_ref(x, c_kv, w_q, w_uk, w_uv, w_o):
        B, Sq, M = (x.shape[0], x.shape[1], x.shape[2])
        Sk = c_kv.shape[1]
        H, D = (w_q.shape[1], w_q.shape[2])
        q = torch.einsum('bqm,mhd->bhqd', x, w_q)
        k = torch.einsum('bkc,chd->bhkd', c_kv, w_uk)
        v = torch.einsum('bkc,chd->bhkd', c_kv, w_uv)
        scores = torch.einsum('bhqd,bhkd->bhqk', q, k) * D ** (-0.5)
        off = Sk - Sq
        qi = torch.arange(Sq)[:, None]
        kj = torch.arange(Sk)[None, :]
        cmask = kj <= qi + off
        scores = torch.where(cmask[None, None], scores, -1000000000.0)
        p = _softmax(scores, axis=-1)
        ctx = torch.einsum('bhqk,bhkd->bhqd', p, v)
        return torch.einsum('bhqd,hdm->bqm', ctx, w_o)

    def _B_ref(x, w_in, w_sp, b_sp, w_out):
        B, S, D = x.shape
        F = w_in.shape[-1]
        Fh = F // 2
        z = _gelu(torch.einsum('bsd,df->bsf', x, w_in))
        z1 = z[..., :Fh].clone()
        z2 = _layernorm(z[..., Fh:], axis=-1)
        g = torch.einsum('ts,bsf->btf', w_sp, z2) + b_sp[None, :, None]
        return torch.einsum('bsf,fd->bsd', z1 * g, w_out)
    x = A_x
    xn = _layernorm(x, axis=-1)
    return x + _A_ref(xn, A_c_kv, A_w_q, A_w_uk, A_w_uv, A_w_o) + _B_ref(xn, B_w_in, B_w_sp, B_b_sp, B_w_out)
```

```cpp
#include <hip/hip_runtime.h>
#include <hip/hip_bf16.h>
#include <stdint.h>

typedef __bf16 bf16x8 __attribute__((ext_vector_type(8)));
typedef __bf16 bf16x4 __attribute__((ext_vector_type(4)));
typedef __bf16 bf16x2 __attribute__((ext_vector_type(2)));
typedef float f32x16 __attribute__((ext_vector_type(16)));
typedef float f32x4 __attribute__((ext_vector_type(4)));
typedef float f32x2 __attribute__((ext_vector_type(2)));
typedef unsigned u32x4 __attribute__((ext_vector_type(4)));
typedef unsigned u32x2 __attribute__((ext_vector_type(2)));
typedef unsigned short u16;
typedef __amdgpu_buffer_rsrc_t rsrc_t;
#define LAS __attribute__((address_space(3)))
#define DI __device__ __forceinline__
#define SB() __builtin_amdgcn_sched_barrier(0)
#define MFMA(a, b, c) __builtin_amdgcn_mfma_f32_32x32x16_bf16((a), (b), (c), 0, 0, 0)
#define WAITV(n) asm volatile("s_waitcnt vmcnt(" #n ")" ::: "memory")
#define WAITL0() asm volatile("s_waitcnt lgkmcnt(0)" ::: "memory")
#define BAR() do { asm volatile("" ::: "memory"); __builtin_amdgcn_s_barrier(); asm volatile("" ::: "memory"); } while (0)

constexpr int SEQ = 2048, DM = 512, NH = 32, HD = 128, FH = 688, FI = 1376, ZLD = 768;
constexpr size_t WS_XN = 0;
constexpr size_t WS_Z1 = WS_XN + (size_t)SEQ * DM * 2;
constexpr size_t WS_Z2 = WS_Z1 + (size_t)SEQ * FH * 2;
constexpr size_t WS_Z2N = WS_Z2 + (size_t)SEQ * FH * 2;
constexpr size_t WS_H = WS_Z2N + (size_t)SEQ * ZLD * 2;
constexpr size_t WS_Q = 32u << 20;
constexpr size_t WS_K = WS_Q + (16u << 20);
constexpr size_t WS_V = WS_K + (16u << 20);
constexpr size_t WS_C = WS_V + (16u << 20);
constexpr size_t WS_P = WS_C + (16u << 20);
static_assert(WS_H + (size_t)SEQ * FH * 2 <= WS_Q, "ws map");

DI unsigned rfl(unsigned v) { return __builtin_amdgcn_readfirstlane(v); }
DI unsigned cvtpk(float lo, float hi) { f32x2 v = {lo, hi}; bf16x2 b = __builtin_convertvector(v, bf16x2); return __builtin_bit_cast(unsigned, b); }
DI float bflo(unsigned u) { return __uint_as_float(u << 16); }
DI float bfhi(unsigned u) { return __uint_as_float(u & 0xffff0000u); }
DI float rbf(float v) { return bflo(cvtpk(v, v) & 0xffffu); }
template <int S> DI u32x4 pack8(const f32x16& c) {
  u32x4 w = {cvtpk(c[8 * S + 0], c[8 * S + 1]), cvtpk(c[8 * S + 2], c[8 * S + 3]), cvtpk(c[8 * S + 4], c[8 * S + 5]), cvtpk(c[8 * S + 6], c[8 * S + 7])};
  return w;
}
DI rsrc_t mkrsrc(const void* p, unsigned bytes) { return __builtin_amdgcn_make_buffer_rsrc((void*)p, (short)0, (int)bytes, 0x00020000); }
template <int IMM> DI void dma16(rsrc_t r, unsigned voff, unsigned soff, unsigned m0v) {
  asm volatile("s_mov_b32 m0, %0\n\ts_nop 0\n\tbuffer_load_dwordx4 %1, %2, %3 offen offset:%4 lds" ::"s"(m0v), "v"(voff), "s"(r), "s"(soff), "i"(IMM) : "memory");
}
DI bf16x8 ldsr(LAS const char* p) { return *(LAS const bf16x8*)p; }
DI bf16x8 ldstr(LAS const char* p0, LAS const char* p1) {
  bf16x4 a = __builtin_amdgcn_ds_read_tr16_b64_v4bf16((LAS bf16x4*)p0);
  bf16x4 b = __builtin_amdgcn_ds_read_tr16_b64_v4bf16((LAS bf16x4*)p1);
  return __builtin_shufflevector(a, b, 0, 1, 2, 3, 4, 5, 6, 7);
}
DI float swap32_add(float v) { auto r = __builtin_amdgcn_permlane32_swap(__float_as_uint(v), __float_as_uint(v), false, false); return __uint_as_float(r[0]) + __uint_as_float(r[1]); }
DI float swap32_max(float v) { auto r = __builtin_amdgcn_permlane32_swap(__float_as_uint(v), __float_as_uint(v), false, false); return fmaxf(__uint_as_float(r[0]), __uint_as_float(r[1])); }
DI float gelu_t(float x) {
  const float u = x * (2.302208198f + 0.1029432397f * x * x);
  return x * __builtin_amdgcn_rcpf(1.f + __builtin_amdgcn_exp2f(-u));
}

constexpr int K0_LDS = 98304 + 4096;
__global__ __launch_bounds__(512, 2) void k0_ln_z(const u16* __restrict__ x, const u16* __restrict__ w_in, char* __restrict__ ws) {
  extern __shared__ __attribute__((aligned(16))) char lds_[];
  LAS char* lds = (LAS char*)lds_;
  const int tid = threadIdx.x, lane = tid & 63, wid = rfl(tid >> 6), r = lane & 31, h = lane >> 5;
  const int rbk = blockIdx.x / 15, cg = blockIdx.x % 15, wr = wid & 3, kh = wid >> 2;
  const int rb32 = rbk * 4 + wr;
  u32x4 xa[16];
  {
    const char* xp = (const char*)x + (size_t)(rb32 * 32 + r) * 1024 + kh * 512 + h * 16;
#pragma unroll
    for (int i = 0; i < 16; ++i) asm volatile("global_load_dwordx4 %0, %1, off offset:%2" : "=v"(xa[i]) : "v"(xp), "i"(i * 32) : "memory");
  }
  {
    const rsrc_t rw = mkrsrc(w_in, DM * FI * 2);
    unsigned vo[3];
#pragma unroll
    for (int u = 0; u < 3; ++u) { const int i = 64 * u + lane; vo[u] = (i / 12) * (FI * 2) + (i % 12) * 16; }
    const unsigned l0 = (unsigned)(size_t)lds;
#pragma unroll
    for (int mm = 0; mm < 4; ++mm) {
      const unsigned m = wid + 8 * mm;
#pragma unroll
      for (int u = 0; u < 3; ++u) dma16<0>(rw, vo[u], m * 16 * (FI * 2) + cg * 192, l0 + (m * 3 + u) * 1024);
    }
  }
  WAITV(12); SB();
  LAS float* st = (LAS float*)(lds + 98304);
  float d[128];
  float s = 0.f;
#pragma unroll
  for (int i = 0; i < 16; ++i)
#pragma unroll
    for (int e = 0; e < 4; ++e) { d[8 * i + 2 * e] = bflo(xa[i][e]); d[8 * i + 2 * e + 1] = bfhi(xa[i][e]); s += d[8 * i + 2 * e] + d[8 * i + 2 * e + 1]; }
  s = swap32_add(s);
  if (h == 0) st[wid * 32 + r] = s;
  WAITL0(); BAR();
  const float mean = (st[wr * 32 + r] + st[(wr + 4) * 32 + r]) * (1.f / 512.f);
  const float mb = rbf(mean);
  float q = 0.f;
#pragma unroll
  for (int i = 0; i < 128; ++i) { d[i] -= mb; q = fmaf(d[i], d[i], q); }
  q = swap32_add(q);
  if (h == 0) st[256 + wid * 32 + r] = q;
  WAITL0(); BAR();
  const float dm = mean - mb;
  const float var = fmaxf((st[256 + wr * 32 + r] + st[256 + (wr + 4) * 32 + r]) * (1.f / 512.f) - dm * dm, 0.f);
  const float rs = 1.f / rbf(sqrtf(rbf(rbf(var) + 1e-5f)));
#pragma unroll
  for (int i = 0; i < 16; ++i)
#pragma unroll
    for (int e = 0; e < 4; ++e) xa[i][e] = cvtpk(d[8 * i + 2 * e] * rs, d[8 * i + 2 * e + 1] * rs);
  if (cg == 0) {
    u32x4* xo = (u32x4*)(ws + WS_XN + ((size_t)rb32 * 32 + kh * 16) * 1024 + lane * 16);
#pragma unroll
    for (int i = 0; i < 16; ++i) xo[i * 64] = xa[i];
  }
  WAITV(0); BAR(); SB();
  f32x16 acc[3] = {};
  {
    const int g = lane >> 4, q4 = (lane >> 2) & 3, p = lane & 3;
    LAS const char* bp = lds + (kh * 256 + 8 * h + q4) * 192 + (g & 1) * 32 + p * 8;
#pragma unroll
    for (int ks = 0; ks < 16; ++ks)
#pragma unroll
      for (int nb = 0; nb < 3; ++nb) {
        const bf16x8 wf = ldstr(bp + ks * 16 * 192 + nb * 64, bp + ks * 16 * 192 + 4 * 192 + nb * 64);
        acc[nb] = MFMA(wf, __builtin_bit_cast(bf16x8, xa[ks]), acc[nb]);
      }
  }
  BAR();
  {
    LAS f32x4* ex = (LAS f32x4*)lds;
#pragma unroll
    for (int nb = 0; nb < 3; ++nb)
#pragma unroll
      for (int e = 0; e < 2; ++e) {
        f32x4 v;
#pragma unroll
        for (int c = 0; c < 4; ++c) v[c] = kh ? acc[nb][4 * e + c] : acc[nb][8 + 4 * e + c];
        ex[((wid * 3 + nb) * 2 + e) * 64 + lane] = v;
      }
    WAITL0(); BAR();
    const int pw = wid ^ 4;
#pragma unroll
    for (int nb = 0; nb < 3; ++nb) {
      float o[8];
#pragma unroll
      for (int e = 0; e < 2; ++e) {
        const f32x4 v = ex[((pw * 3 + nb) * 2 + e) * 64 + lane];
#pragma unroll
        for (int c = 0; c < 4; ++c) o[4 * e + c] = gelu_t(v[c] + (kh ? acc[nb][8 + 4 * e + c] : acc[nb][4 * e + c]));
      }
      const u32x4 w = {cvtpk(o[0], o[1]), cvtpk(o[2], o[3]), cvtpk(o[4], o[5]), cvtpk(o[6], o[7])};
      const int k16 = 2 * (3 * cg + nb) + kh;
      if (k16 < 43) *(u32x4*)(ws + WS_Z1 + ((size_t)rb32 * 43 + k16) * 1024 + lane * 16) = w;
      else if (k16 < 86) {
        char* zp = ws + WS_Z2 + (size_t)(rb32 * 32 + r) * (FH * 2) + (k16 - 43) * 32 + h * 8;
        *(u32x2*)zp = (u32x2){w[0], w[1]};
        *(u32x2*)(zp + 16) = (u32x2){w[2], w[3]};
      }
    }
  }
}

constexpr int K1_LDS = 131072;
constexpr float QSCALE = 0.08838834764831845f * 1.4426950408889634f;
struct K1C {
  rsrc_t rA, rW0, rW1, rXn, rWq;
  unsigned voA[2], voB, voX, ldsb, m0A, m0B, soA, soB, soX;
  LAS const char* aR[4]; LAS const char* aX; LAS const char* bR;
};
template <int ST, int I, bool QN> DI void k1_dma(const K1C& c, int t) {
  if constexpr (!QN) {
    if constexpr (I < 4) dma16<0>(c.rA, c.voA[I & 1], c.soA + t * 128 + I * 8192, c.m0A + ST * 32768 + I * 1024);
    else if constexpr (I < 6) dma16<0>(c.rW0, c.voB, c.soB + t * 524288 + (I - 4) * 32768, c.m0B + ST * 32768 + (I - 4) * 1024);
    else dma16<0>(c.rW1, c.voB, c.soB + t * 524288 + (I - 6) * 32768, c.m0B + ST * 32768 + 16384 + (I - 6) * 1024);
  } else {
    if constexpr (I < 4) dma16<I * 1024>(c.rXn, c.voX, c.soX + (t - 8) * 4096, c.m0A + ST * 32768);
    else if constexpr (I < 6) dma16<0>(c.rWq, c.voB, c.soB + (t - 8) * 524288 + (I - 4) * 32768, c.m0B + ST * 32768 + (I - 4) * 1024);
  }
}
struct K1F { bf16x8 a[4], w0, w1; };
template <int ST, int KS, bool Q> DI void k1_rd(const K1C& c, K1F& f) {
#pragma unroll
  for (int i = 0; i < 4; ++i) f.a[i] = Q ? ldsr(c.aX + ST * 32768 + i * 4096 + KS * 1024) : ldsr(c.aR[KS] + ST * 32768 + i * 4096);
  f.w0 = ldstr(c.bR + ST * 32768 + KS * 4096, c.bR + ST * 32768 + KS * 4096 + 1024);
  if constexpr (!Q) f.w1 = ldstr(c.bR + ST * 32768 + 16384 + KS * 4096, c.bR + ST * 32768 + 16384 + KS * 4096 + 1024);
}
template <bool Q> DI void k1_mm(const K1F& f, f32x16 (&a0)[4], f32x16 (&a1)[4], int lo, int hi) {
#pragma unroll
  for (int i = 0; i < 4; ++i) if (i >= lo && i < hi) { a0[i] = MFMA(f.w0, f.a[i], a0[i]); if constexpr (!Q) a1[i] = MFMA(f.a[i], f.w1, a1[i]); }
}
template <int ST, bool Q, bool QN1, bool QN2, bool DM2, bool RD1, int VW> DI void k1_tile(const K1C& c, int t, K1F& f0, K1F& f1, f32x16 (&a0)[4], f32x16 (&a1)[4]) {
  k1_rd<ST, 1, Q>(c, f1); SB(); k1_mm<Q>(f0, a0, a1, 0, 4); SB();
  k1_rd<ST, 2, Q>(c, f0); SB(); k1_mm<Q>(f1, a0, a1, 0, 4); SB();
  k1_rd<ST, 3, Q>(c, f1); SB(); k1_mm<Q>(f0, a0, a1, 0, 4); SB();
  asm volatile("s_waitcnt vmcnt(%0) lgkmcnt(0)" ::"i"(VW) : "memory"); BAR(); SB();
  if constexpr (RD1) k1_rd<ST ^ 1, 0, QN1>(c, f0);
  SB();
  k1_mm<Q>(f1, a0, a1, 0, 1); SB(); if constexpr (DM2) { k1_dma<ST, 0, QN2>(c, t + 2); k1_dma<ST, 1, QN2>(c, t + 2); } SB();
  k1_mm<Q>(f1, a0, a1, 1, 2); SB(); if constexpr (DM2) { k1_dma<ST, 2, QN2>(c, t + 2); k1_dma<ST, 3, QN2>(c, t + 2); } SB();
  k1_mm<Q>(f1, a0, a1, 2, 3); SB(); if constexpr (DM2) { k1_dma<ST, 4, QN2>(c, t + 2); k1_dma<ST, 5, QN2>(c, t + 2); } SB();
  k1_mm<Q>(f1, a0, a1, 3, 4); SB(); if constexpr (DM2) { k1_dma<ST, 6, QN2>(c, t + 2); k1_dma<ST, 7, QN2>(c, t + 2); } SB();
}
__global__ __launch_bounds__(512, 2) void k1_qkv(const u16* __restrict__ ckv, const u16* __restrict__ wq, const u16* __restrict__ wk, const u16* __restrict__ wv, char* __restrict__ ws) {
  extern __shared__ __attribute__((aligned(16))) char lds_[];
  LAS char* lds = (LAS char*)lds_;
  const int tid = threadIdx.x, lane = tid & 63, wid = rfl(tid >> 6), r = lane & 31, h = lane >> 5;
  const int bid = blockIdx.x, idx = bid >> 3, head = (bid & 7) * 4 + (idx >> 3), rb = idx & 7;
  const int wm = wid >> 2, wn = wid & 3;
  K1C c;
  c.rA = mkrsrc(ckv, SEQ * DM * 2); c.rW0 = mkrsrc(wk, DM * 4096 * 2); c.rW1 = mkrsrc(wv, DM * 4096 * 2); c.rWq = mkrsrc(wq, DM * 4096 * 2);
  c.rXn = mkrsrc(ws + WS_XN, SEQ * DM * 2);
  c.ldsb = (unsigned)(size_t)lds;
#pragma unroll
  for (int p = 0; p < 2; ++p) c.voA[p] = (lane >> 3) * 1024 + (((lane & 7) ^ (4 * p + (lane >> 4))) * 16);
  c.voB = (lane >> 4) * 8192 + (((lane & 15) ^ ((lane >> 4) << 2)) * 16);
  c.voX = lane * 16;
  c.soA = (rb * 256 + wid * 32) * 1024; c.soB = wid * 8 * 8192 + head * 256; c.soX = (rb * 8 + wid) * 32768;
  c.m0A = c.ldsb + wid * 4096; c.m0B = c.ldsb + 65536 + wid * 2048;
  {
    const int tt = h ^ ((r >> 1) & 7);
#pragma unroll
    for (int ks = 0; ks < 4; ++ks) c.aR[ks] = lds + (wm * 128 + r) * 128 + ((tt ^ (2 * ks)) * 16);
    c.aX = lds + wm * 16384 + lane * 16;
    const int g = lane >> 4, q4 = (lane >> 2) & 3, p = lane & 3;
    c.bR = lds + 65536 + (8 * h + q4) * 256 + ((wn ^ q4) * 64) + (g & 1) * 32 + p * 8;
  }
  k1_dma<0, 0, false>(c, 0); k1_dma<0, 1, false>(c, 0); k1_dma<0, 2, false>(c, 0); k1_dma<0, 3, false>(c, 0);
  k1_dma<0, 4, false>(c, 0); k1_dma<0, 5, false>(c, 0); k1_dma<0, 6, false>(c, 0); k1_dma<0, 7, false>(c, 0);
  k1_dma<1, 0, false>(c, 1); k1_dma<1, 1, false>(c, 1); k1_dma<1, 2, false>(c, 1); k1_dma<1, 3, false>(c, 1);
  k1_dma<1, 4, false>(c, 1); k1_dma<1, 5, false>(c, 1); k1_dma<1, 6, false>(c, 1); k1_dma<1, 7, false>(c, 1);
  {
    const int row = bid * 8 + wid;
    const u32x4* zp = (const u32x4*)(ws + WS_Z2 + (size_t)row * (FH * 2));
    const bool act = lane < 43;
    u32x4 v0 = {}, v1 = {};
    if (act) { v0 = zp[lane]; v1 = zp[lane + 43]; }
    float d[16]; float s = 0.f;
#pragma unroll
    for (int e = 0; e < 4; ++e) { d[2 * e] = bflo(v0[e]); d[2 * e + 1] = bfhi(v0[e]); d[8 + 2 * e] = bflo(v1[e]); d[9 + 2 * e] = bfhi(v1[e]); }
#pragma unroll
    for (int e = 0; e < 16; ++e) s += d[e];
#pragma unroll
    for (int o = 1; o < 64; o <<= 1) s += __shfl_xor(s, o);
    const float mean = s * (1.f / FH), mb = rbf(mean);
    float q = 0.f;
#pragma unroll
    for (int e = 0; e < 16; ++e) { d[e] -= mb; q = fmaf(d[e], d[e], q); }
    if (!act) q = 0.f;
#pragma unroll
    for (int o = 1; o < 64; o <<= 1) q += __shfl_xor(q, o);
    const float dm = mean - mb, var = fmaxf(q * (1.f / FH) - dm * dm, 0.f);
    const float rs = 1.f / rbf(sqrtf(rbf(rbf(var) + 1e-5f)));
    const int prow = (row & ~12) | ((row & 4) << 1) | ((row & 8) >> 1);
    u32x4* op = (u32x4*)(ws + WS_Z2N + (size_t)prow * (ZLD * 2));
    if (act) {
#pragma unroll
      for (int e = 0; e < 4; ++e) { v0[e] = cvtpk(d[2 * e] * rs, d[2 * e + 1] * rs); v1[e] = cvtpk(d[8 + 2 * e] * rs, d[9 + 2 * e] * rs); }
      op[lane] = v0; op[lane + 43] = v1;
    } else if (lane < 53) op[lane + 43] = (u32x4){0u, 0u, 0u, 0u};
  }
  f32x16 a0[4] = {}, a1[4] = {};
  K1F f0, f1;
  WAITV(8); BAR(); SB();
  k1_rd<0, 0, false>(c, f0); SB();
  for (int t = 0; t < 6; t += 2) {
    k1_tile<0, false, false, false, true, true, 0>(c, t, f0, f1, a0, a1);
    k1_tile<1, false, false, false, true, true, 0>(c, t + 1, f0, f1, a0, a1);
  }
  k1_tile<0, false, false, true, true, true, 0>(c, 6, f0, f1, a0, a1);
  k1_tile<1, false, true, true, true, true, 0>(c, 7, f0, f1, a0, a1);
  {
    const size_t o = ((size_t)(head * 64 + rb * 8 + wm * 4) * 8 + 2 * wn) * 1024 + lane * 16;
#pragma unroll
    for (int i = 0; i < 4; ++i) {
      *(u32x4*)(ws + WS_K + o + i * 8192) = pack8<0>(a0[i]); *(u32x4*)(ws + WS_K + o + i * 8192 + 1024) = pack8<1>(a0[i]);
      *(u32x4*)(ws + WS_V + o + i * 8192) = pack8<0>(a1[i]); *(u32x4*)(ws + WS_V + o + i * 8192 + 1024) = pack8<1>(a1[i]);
    }
#pragma unroll
    for (int i = 0; i < 4; ++i) a0[i] = (f32x16){};
  }
  SB();
  k1_tile<0, true, true, true, true, true, 16>(c, 8, f0, f1, a0, a1);
  k1_tile<1, true, true, true, true, true, 0>(c, 9, f0, f1, a0, a1);
  for (int t = 10; t < 14; t += 2) {
    k1_tile<0, true, true, true, true, true, 0>(c, t, f0, f1, a0, a1);
    k1_tile<1, true, true, true, true, true, 0>(c, t + 1, f0, f1, a0, a1);
  }
  k1_tile<0, true, true, true, false, true, 0>(c, 14, f0, f1, a0, a1);
  k1_tile<1, true, true, true, false, false, 0>(c, 15, f0, f1, a0, a1);
  {
    const size_t o = ((size_t)(head * 64 + rb * 8 + wm * 4) * 8 + 2 * wn) * 1024 + lane * 16;
#pragma unroll
    for (int i = 0; i < 4; ++i) {
      a0[i] *= QSCALE;
      *(u32x4*)(ws + WS_Q + o + i * 8192) = pack8<0>(a0[i]); *(u32x4*)(ws + WS_Q + o + i * 8192 + 1024) = pack8<1>(a0[i]);
    }
  }
}

constexpr int K2_LDS = 163840;
constexpr float THR = 8.f;
struct K2C {
  rsrc_t rK, rV;
  unsigned vo, m0g, sog;
  LAS const char* rd;
  int n1;
};
DI unsigned k2_tile_off(const K2C& c, int n) { n = n > 16 ? 16 : n; const int i = n < c.n1 ? n : n - c.n1; return c.sog + (unsigned)i * 32768u; }
template <int OFF> DI void k2_dma4(rsrc_t r, const K2C& c, unsigned so) {
  dma16<0>(r, c.vo, so, c.m0g + OFF); dma16<1024>(r, c.vo, so, c.m0g + OFF); dma16<2048>(r, c.vo, so, c.m0g + OFF); dma16<3072>(r, c.vo, so, c.m0g + OFF);
}
template <int SLOT> DI void k2_qk(const K2C& c, const u32x4 (&q)[8], const f32x16& init, f32x16 (&s)[2]) {
#pragma unroll
  for (int kb = 0; kb < 2; ++kb)
#pragma unroll
    for (int ks = 0; ks < 8; ++ks) {
      const bf16x8 kf = ldsr(c.rd + SLOT * 16384 + kb * 8192 + ks * 1024);
      s[kb] = MFMA(kf, __builtin_bit_cast(bf16x8, q[ks]), ks == 0 ? init : s[kb]);
    }
}
DI void k2_mask(f32x16 (&s)[2], int dq) {
#pragma unroll
  for (int kb = 0; kb < 2; ++kb)
#pragma unroll
    for (int e = 0; e < 16; ++e) { const int cc = 32 * kb + (e & 3) + 8 * (e >> 2); if (cc > dq) s[kb][e] = -__builtin_inff(); }
}
DI float k2_max(const f32x16 (&s)[2]) {
  float m0 = fmaxf(s[0][0], s[0][1]), m1 = fmaxf(s[1][0], s[1][1]);
#pragma unroll
  for (int e = 2; e < 16; e += 2) { m0 = fmaxf(fmaxf(m0, s[0][e]), s[0][e + 1]); m1 = fmaxf(fmaxf(m1, s[1][e]), s[1][e + 1]); }
  return fmaxf(m0, m1);
}
DI void k2_loadq(u32x4 (&q)[8], const char* p) {
#pragma unroll
  for (int i = 0; i < 4; ++i) asm volatile("global_load_dwordx4 %0, %1, off offset:%2" : "+v"(q[i]) : "v"(p), "i"(i * 1024) : "memory");
#pragma unroll
  for (int i = 0; i < 4; ++i) asm volatile("global_load_dwordx4 %0, %1, off offset:%2" : "+v"(q[4 + i]) : "v"(p + 4096), "i"(i * 1024) : "memory");
}
struct K2S { f32x16 o[4], negm; u32x4 q[8]; float m, l, mo; };

DI void k2_rescale(K2S& S, f32x16 (&sn)[2], float lmax, bool first) {
  float dl = swap32_max(lmax);
  if (first) { dl = dl == -__builtin_inff() ? 0.f : dl; S.mo = S.m; S.m = dl; }
  else {
    dl = fmaxf(dl, 0.f); S.m += dl;
    const float f = __builtin_amdgcn_exp2f(-dl);
    S.l *= f;
#pragma unroll
    for (int db = 0; db < 4; ++db) S.o[db] *= f;
  }
#pragma unroll
  for (int kb = 0; kb < 2; ++kb) sn[kb] -= dl;
#pragma unroll
  for (int e = 0; e < 16; ++e) S.negm[e] = -S.m;
}
DI void k2_finish(K2S& S, float mblk, LAS char* lds, int wid, int lane, char* ctx_base) {
  const int g = wid >> 2, pw = wid ^ 4;
  LAS f32x4* ex = (LAS f32x4*)(lds + 131072);
  const float lt = swap32_add(S.l);
  const float me = lt > 0.f ? mblk : -1e30f;
  f32x16 rcv[2];
  float pm, pl;
#pragma unroll
  for (int rd = 0; rd < 2; ++rd) {
    const f32x16 snd = g ? S.o[rd] : S.o[2 + rd];
#pragma unroll
    for (int e = 0; e < 4; ++e) ex[(wid * 4 + e) * 64 + lane] = (f32x4){snd[4 * e], snd[4 * e + 1], snd[4 * e + 2], snd[4 * e + 3]};
    WAITL0(); BAR();
#pragma unroll
    for (int e = 0; e < 4; ++e) { const f32x4 v = ex[(pw * 4 + e) * 64 + lane]; rcv[rd][4 * e] = v[0]; rcv[rd][4 * e + 1] = v[1]; rcv[rd][4 * e + 2] = v[2]; rcv[rd][4 * e + 3] = v[3]; }
    WAITL0(); BAR();
  }
  {
    LAS f32x2* es = (LAS f32x2*)(lds + 131072);
    es[wid * 64 + lane] = (f32x2){me, lt};
    WAITL0(); BAR();
    const f32x2 v = es[pw * 64 + lane]; pm = v[0]; pl = v[1];
    WAITL0(); BAR();
  }
  const float mm = fmaxf(me, pm);
  float w0 = __builtin_amdgcn_exp2f(me - mm), w1 = __builtin_amdgcn_exp2f(pm - mm);
  const float inv = __builtin_amdgcn_rcpf(w0 * lt + w1 * pl);
  w0 *= inv; w1 *= inv;
#pragma unroll
  for (int rd = 0; rd < 2; ++rd) {
    const f32x16 own = g ? S.o[2 + rd] : S.o[rd];
    const f32x16 res = own * w0 + rcv[rd] * w1;
    char* p = ctx_base + (2 * (2 * g + rd)) * 1024;
    *(u32x4*)p = pack8<0>(res); *(u32x4*)(p + 1024) = pack8<1>(res);
  }
#pragma unroll
  for (int db = 0; db < 4; ++db) S.o[db] = (f32x16){};
  S.l = 0.f;
}
template <int PAR, bool NEXT> DI void k2_step(const K2C& c, K2S& S, int n, f32x16 (&sc)[2], f32x16 (&sn)[2], LAS char* lds, int wid, int lane,
                                              const char* qnext, char* ctx1, char* ctx2, int dq0) {
  if constexpr (NEXT) k2_qk<PAR ^ 1>(c, S.q, S.negm, sn);
  u32x4 p[4];
  {
    float l0 = 0.f, l1 = 0.f;
#pragma unroll
    for (int kb = 0; kb < 2; ++kb)
#pragma unroll
      for (int e = 0; e < 16; e += 2) { sc[kb][e] = __builtin_amdgcn_exp2f(sc[kb][e]); sc[kb][e + 1] = __builtin_amdgcn_exp2f(sc[kb][e + 1]); l0 += sc[kb][e]; l1 += sc[kb][e + 1]; }
    S.l += l0 + l1;
    p[0] = pack8<0>(sc[0]); p[1] = pack8<1>(sc[0]); p[2] = pack8<0>(sc[1]); p[3] = pack8<1>(sc[1]);
  }
  SB();
  if constexpr (NEXT) { if (n + 2 == c.n1) k2_loadq(S.q, qnext); }
  bf16x8 vf[4];
#pragma unroll
  for (int ks = 0; ks < 3; ++ks)
#pragma unroll
    for (int db = 0; db < 4; ++db) {
      const bf16x8 v = ldsr(c.rd + 32768 + PAR * 16384 + (ks >> 1) * 8192 + db * 2048 + (ks & 1) * 1024);
      S.o[db] = MFMA(v, __builtin_bit_cast(bf16x8, p[ks]), S.o[db]);
    }
#pragma unroll
  for (int db = 0; db < 4; ++db) vf[db] = ldsr(c.rd + 32768 + PAR * 16384 + 8192 + db * 2048 + 1024);
  float lmax = 0.f;
  const bool first = n + 1 == c.n1;
  if constexpr (NEXT) {
    if (n + 2 == c.n1 || n + 1 == 16) k2_mask(sn, dq0);
    lmax = k2_max(sn);
  }
  SB();
  asm volatile("s_waitcnt vmcnt(0) lgkmcnt(0)" ::: "memory"); BAR(); SB();
  if constexpr (NEXT) { k2_dma4<(PAR ^ 1) * 16384>(c.rK, c, k2_tile_off(c, n + 3)); }
  SB();
#pragma unroll
  for (int db = 0; db < 4; ++db) S.o[db] = MFMA(vf[db], __builtin_bit_cast(bf16x8, p[3]), S.o[db]);
  SB();
  if constexpr (NEXT) { k2_dma4<32768 + PAR * 16384>(c.rV, c, k2_tile_off(c, n + 2)); }
  SB();
  if constexpr (NEXT) {
    if (first || __any(lmax > THR)) k2_rescale(S, sn, lmax, first);
    if (first) k2_finish(S, S.mo, lds, wid, lane, ctx1);
    if (n + 2 == c.n1) {
#pragma unroll
      for (int e = 0; e < 16; ++e) S.negm[e] = 0.f;
    }
  } else k2_finish(S, S.m, lds, wid, lane, ctx2);
  SB();
}
__global__ __launch_bounds__(512, 2) void k2_attn(char* __restrict__ ws) {
  extern __shared__ __attribute__((aligned(16))) char lds_[];
  LAS char* lds = (LAS char*)lds_;
  const int tid = threadIdx.x, lane = tid & 63, wid = rfl(tid >> 6), r = lane & 31, h = lane >> 5;
  const int bid = blockIdx.x, idx = bid >> 3, head = (bid & 7) * 4 + (idx >> 3), j = idx & 7;
  const int g = wid >> 2, wq = wid & 3;
  K2C c;
  c.rK = mkrsrc(ws + WS_K + (size_t)head * 524288, 524288); c.rV = mkrsrc(ws + WS_V + (size_t)head * 524288, 524288);
  c.vo = lane * 16; c.m0g = (unsigned)(size_t)lds + g * 65536 + wq * 4096; c.sog = g * 16384 + wq * 4096;
  c.rd = lds + g * 65536 + lane * 16; c.n1 = 16 - j;
  const int rbA = (15 - j) * 4 + wq, rbB = j * 4 + wq;
  const char* qA = ws + WS_Q + (size_t)(head * 64 + rbA) * 8192 + lane * 16;
  const char* qB = ws + WS_Q + (size_t)(head * 64 + rbB) * 8192 + lane * 16;
  char* ctx1 = ws + WS_C + (size_t)(rbA * 32 + head) * 8192 + lane * 16;
  char* ctx2 = ws + WS_C + (size_t)(rbB * 32 + head) * 8192 + lane * 16;
  const int dq0 = wq * 32 + r - g * 64 - 4 * h;
  K2S S;
#pragma unroll
  for (int i = 0; i < 8; ++i) S.q[i] = (u32x4){};
  k2_loadq(S.q, qA);
  k2_dma4<0>(c.rK, c, k2_tile_off(c, 0));
  k2_dma4<32768>(c.rV, c, k2_tile_off(c, 0));
  k2_dma4<16384>(c.rK, c, k2_tile_off(c, 1));
#pragma unroll
  for (int db = 0; db < 4; ++db) S.o[db] = (f32x16){};
  S.negm = (f32x16){}; S.m = 0.f; S.l = 0.f; S.mo = 0.f;
  f32x16 sa[2], sb[2];
  WAITV(8); BAR(); SB();
  k2_qk<0>(c, S.q, S.negm, sa);
  k2_rescale(S, sa, k2_max(sa), true);
  SB();
  asm volatile("s_waitcnt vmcnt(0) lgkmcnt(0)" ::: "memory"); BAR(); SB();
  k2_dma4<0>(c.rK, c, k2_tile_off(c, 2));
  k2_dma4<32768 + 16384>(c.rV, c, k2_tile_off(c, 1));
  for (int n = 0; n < 16; n += 2) {
    k2_step<0, true>(c, S, n, sa, sb, lds, wid, lane, qB, ctx1, ctx2, dq0);
    k2_step<1, true>(c, S, n + 1, sb, sa, lds, wid, lane, qB, ctx1, ctx2, dq0);
  }
  k2_step<0, false>(c, S, 16, sa, sb, lds, wid, lane, qB, ctx1, ctx2, dq0);
}

constexpr int K3_LDS = 131072;
__global__ __launch_bounds__(512, 2) void k3_gemm(const u16* __restrict__ wo, const u16* __restrict__ wsp, const u16* __restrict__ bsp, char* __restrict__ ws) {
  extern __shared__ __attribute__((aligned(16))) char lds_[];
  LAS char* lds = (LAS char*)lds_;
  const int tid = threadIdx.x, lane = tid & 63, wid = rfl(tid >> 6), r = lane & 31, h = lane >> 5;
  const int bid = blockIdx.x, xcd = bid & 7, idx = bid >> 3;
  const bool isA = idx < 16;
  const int wm = wid >> 2, wn = wid & 3;
  int rb, nb, sp = 0;
  if (isA) { sp = xcd & 1; rb = (xcd >> 1) * 4 + (idx >> 2); nb = idx & 3; } else { rb = xcd * 2 + (idx - 16) / 6; nb = (idx - 16) % 6; }
  rsrc_t rA, rB; unsigned voA[2], soA, stA, voB, soB, stB, ldb;
  LAS const char* aR[4];
  if (isA) {
    rA = mkrsrc(ws + WS_C, 16u << 20); rB = mkrsrc(wo, 4096 * DM * 2); ldb = 1024;
    voA[0] = lane * 16; voA[1] = lane * 16 + 1024; soA = ((rb * 4 + (wid >> 1)) * 64 + sp * 32) * 4096 + (wid & 1) * 2048; stA = 4096;
    soB = sp * 2048 * 1024 + nb * 256;
#pragma unroll
    for (int ks = 0; ks < 4; ++ks) aR[ks] = lds + wm * 8192 + ks * 1024 + lane * 16;
  } else {
    rA = mkrsrc(wsp, SEQ * SEQ * 2); rB = mkrsrc(ws + WS_Z2N, SEQ * ZLD * 2); ldb = ZLD * 2;
#pragma unroll
    for (int i = 0; i < 2; ++i) voA[i] = (8 * i + (lane >> 3)) * 4096 + (((lane & 7) ^ (4 * i + (lane >> 4))) * 16);
    soA = (rb * 128 + wid * 16) * 4096; stA = 128;
    soB = nb * 256;
    const int tt = h ^ ((r >> 1) & 7);
#pragma unroll
    for (int ks = 0; ks < 4; ++ks) aR[ks] = lds + (wm * 64 + r) * 128 + ((tt ^ (2 * ks)) * 16);
  }
  voB = (lane >> 4) * ldb + (((lane & 15) ^ ((lane >> 4) << 2)) * 16);
  soB += wid * 8 * ldb; stB = 64 * ldb;
  const unsigned sB1 = 4 * ldb, m0 = (unsigned)(size_t)lds + wid * 2048;
  const int g = lane >> 4, q4 = (lane >> 2) & 3, p = lane & 3;
  LAS const char* bR = lds + 16384 + (4 * h + q4) * 256 + ((wn ^ q4) * 64) + (g & 1) * 32 + p * 8;
#define K3_DMA(ST, t) do { dma16<0>(rA, voA[0], soA + (t) * stA, m0 + (ST) * 32768); dma16<0>(rA, voA[1], soA + (t) * stA, m0 + (ST) * 32768 + 1024); \
    dma16<0>(rB, voB, soB + (t) * stB, m0 + (ST) * 32768 + 16384); dma16<0>(rB, voB, soB + (t) * stB + sB1, m0 + (ST) * 32768 + 16384 + 1024); } while (0)
#define K3_TILE(ST, t) do { WAITV(8); BAR(); SB(); { const int tn = (t) + 3 < 32 ? (t) + 3 : 31; K3_DMA(((ST) + 3) & 3, tn); } SB(); \
    _Pragma("unroll") for (int ks = 0; ks < 4; ++ks) { \
      LAS const char* ap = aR[ks] + ((ST) & 1) * 32768 + ((ST) >> 1) * 65536; LAS const char* bp = bR + ((ST) & 1) * 32768 + ((ST) >> 1) * 65536 + ks * 4096; \
      const bf16x8 a0 = ldsr(ap), a1 = ldsr(ap + 4096), bf = ldstr(bp, bp + 2048); \
      acc[0] = MFMA(bf, a0, acc[0]); acc[1] = MFMA(bf, a1, acc[1]); } SB(); } while (0)
  f32x16 acc[2] = {};
  K3_DMA(0, 0); K3_DMA(1, 1); K3_DMA(2, 2);
  for (int t = 0; t < 32; t += 4) { K3_TILE(0, t); K3_TILE(1, t + 1); K3_TILE(2, t + 2); K3_TILE(3, t + 3); }
  WAITV(0);
  if (isA) {
#pragma unroll
    for (int i = 0; i < 2; ++i) {
      f32x4* pp = (f32x4*)(ws + WS_P + ((size_t)((sp * 64 + rb * 4 + wm * 2 + i) * 16 + nb * 4 + wn) * 4) * 1024 + lane * 16);
#pragma unroll
      for (int e = 0; e < 4; ++e) pp[e * 64] = (f32x4){acc[i][4 * e], acc[i][4 * e + 1], acc[i][4 * e + 2], acc[i][4 * e + 3]};
    }
  } else {
#pragma unroll
    for (int i = 0; i < 2; ++i) {
      const int rb32 = rb * 4 + wm * 2 + i;
      const float b = bflo(bsp[rb32 * 32 + r]);
#pragma unroll
      for (int s = 0; s < 2; ++s) {
        const int k16 = 2 * (nb * 4 + wn) + s;
        if (k16 < 43) {
          const size_t o = ((size_t)rb32 * 43 + k16) * 1024 + lane * 16;
          const u32x4 z = *(const u32x4*)(ws + WS_Z1 + o);
          u32x4 w;
#pragma unroll
          for (int e = 0; e < 4; ++e) w[e] = cvtpk(bflo(z[e]) * (acc[i][8 * s + 2 * e] + b), bfhi(z[e]) * (acc[i][8 * s + 2 * e + 1] + b));
          *(u32x4*)(ws + WS_H + o) = w;
        }
      }
    }
  }
}

constexpr int K4_LDS = 88064;
__global__ __launch_bounds__(256, 1) void k4_out(const u16* __restrict__ x, const u16* __restrict__ wout, const char* __restrict__ ws, u16* __restrict__ out) {
  extern __shared__ __attribute__((aligned(16))) char lds_[];
  LAS char* lds = (LAS char*)lds_;
  const int tid = threadIdx.x, lane = tid & 63, wid = rfl(tid >> 6), r = lane & 31, h = lane >> 5;
  const int bid = blockIdx.x, rb = (bid & 7) * 4 + ((bid >> 3) & 3), cb = bid >> 5;
  const int wm = wid >> 1, wn = wid & 1, rb32 = rb * 2 + wm, nb32 = cb * 2 + wn;
  {
    const rsrc_t rw = mkrsrc(wout, FH * DM * 2);
    const unsigned vo = (lane >> 3) * 1024 + (((lane & 7) ^ (((lane >> 4) & 1) << 2)) * 16), l0 = (unsigned)(size_t)lds;
    for (int pc = wid; pc < 86; pc += 4) dma16<0>(rw, vo, pc * 8192 + cb * 128, l0 + pc * 1024);
  }
  u32x4 hf[43];
  const u32x4* hp = (const u32x4*)(ws + WS_H + (size_t)rb32 * 43 * 1024 + lane * 16);
#pragma unroll
  for (int i = 0; i < 43; ++i) hf[i] = hp[i * 64];
  f32x16 acc;
  {
    const f32x4* p0 = (const f32x4*)(ws + WS_P + ((size_t)(rb32 * 16 + nb32) * 4) * 1024 + lane * 16);
    const f32x4* p1 = (const f32x4*)((const char*)p0 + (4u << 20));
    const char* xp = (const char*)x + (size_t)(rb32 * 32 + r) * 1024 + nb32 * 64 + h * 8;
#pragma unroll
    for (int e = 0; e < 4; ++e) {
      const f32x4 a = p0[e * 64], b = p1[e * 64]; const u32x2 xv = *(const u32x2*)(xp + e * 16);
      acc[4 * e] = a[0] + b[0] + bflo(xv[0]); acc[4 * e + 1] = a[1] + b[1] + bfhi(xv[0]); acc[4 * e + 2] = a[2] + b[2] + bflo(xv[1]); acc[4 * e + 3] = a[3] + b[3] + bfhi(xv[1]);
    }
  }
  WAITV(0); BAR(); SB();
  const int g = lane >> 4, q4 = (lane >> 2) & 3, p = lane & 3;
  LAS const char* bR = lds + (4 * h + q4) * 128 + ((wn ^ (q4 >> 1)) * 64) + (g & 1) * 32 + p * 8;
#pragma unroll
  for (int ks = 0; ks < 43; ++ks) {
    LAS const char* bp = bR + (ks & 15) * 2048 + (ks >> 4) * 32768;
    acc = MFMA(ldstr(bp, bp + 1024), __builtin_bit_cast(bf16x8, hf[ks]), acc);
  }
  char* op = (char*)out + (size_t)(rb32 * 32 + r) * 1024 + nb32 * 64 + h * 8;
#pragma unroll
  for (int e = 0; e < 4; ++e) *(u32x2*)(op + e * 16) = (u32x2){cvtpk(acc[4 * e], acc[4 * e + 1]), cvtpk(acc[4 * e + 2], acc[4 * e + 3])};
}

extern "C" void kernel_launch(void* const* d_in, const int* in_sizes, int n_in, void* d_out, int out_size, void* d_ws, size_t ws_size, hipStream_t stream) {
  (void)in_sizes; (void)n_in; (void)out_size; (void)ws_size;
  const u16* x = (const u16*)d_in[0]; const u16* ckv = (const u16*)d_in[1]; const u16* wq = (const u16*)d_in[2]; const u16* wk = (const u16*)d_in[3];
  const u16* wv = (const u16*)d_in[4]; const u16* wo = (const u16*)d_in[5]; const u16* w_in = (const u16*)d_in[7]; const u16* wsp = (const u16*)d_in[8];
  const u16* bsp = (const u16*)d_in[9]; const u16* wout = (const u16*)d_in[10];
  char* ws = (char*)d_ws;
  (void)hipFuncSetAttribute((const void*)k0_ln_z, hipFuncAttributeMaxDynamicSharedMemorySize, K0_LDS);
  (void)hipFuncSetAttribute((const void*)k1_qkv, hipFuncAttributeMaxDynamicSharedMemorySize, K1_LDS);
  (void)hipFuncSetAttribute((const void*)k2_attn, hipFuncAttributeMaxDynamicSharedMemorySize, K2_LDS);
  (void)hipFuncSetAttribute((const void*)k3_gemm, hipFuncAttributeMaxDynamicSharedMemorySize, K3_LDS);
  (void)hipFuncSetAttribute((const void*)k4_out, hipFuncAttributeMaxDynamicSharedMemorySize, K4_LDS);
  k0_ln_z<<<240, 512, K0_LDS, stream>>>(x, w_in, ws);
  k1_qkv<<<256, 512, K1_LDS, stream>>>(ckv, wq, wk, wv, ws);
  k2_attn<<<256, 512, K2_LDS, stream>>>(ws);
  k3_gemm<<<224, 512, K3_LDS, stream>>>(wo, wsp, bsp, ws);
  k4_out<<<256, 256, K4_LDS, stream>>>(x, wout, ws, (u16*)d_out);
}
```

```cpp
#include <hip/hip_runtime.h>
#include <hip/hip_bf16.h>
#include <stdint.h>

typedef __bf16 bf16x8 __attribute__((ext_vector_type(8)));
typedef __bf16 bf16x4 __attribute__((ext_vector_type(4)));
typedef __bf16 bf16x2 __attribute__((ext_vector_type(2)));
typedef float f32x16 __attribute__((ext_vector_type(16)));
typedef float f32x4 __attribute__((ext_vector_type(4)));
typedef float f32x2 __attribute__((ext_vector_type(2)));
typedef unsigned u32x4 __attribute__((ext_vector_type(4)));
typedef unsigned u32x2 __attribute__((ext_vector_type(2)));
typedef unsigned short u16;
typedef __amdgpu_buffer_rsrc_t rsrc_t;
#define LAS __attribute__((address_space(3)))
#define DI __device__ __forceinline__
#define SB() __builtin_amdgcn_sched_barrier(0)
#define MFMA(a, b, c) __builtin_amdgcn_mfma_f32_32x32x16_bf16((a), (b), (c), 0, 0, 0)
#define WAITV(n) asm volatile("s_waitcnt vmcnt(" #n ")" ::: "memory")
#define WAITL0() asm volatile("s_waitcnt lgkmcnt(0)" ::: "memory")
#define BAR() do { asm volatile("" ::: "memory"); __builtin_amdgcn_s_barrier(); asm volatile("" ::: "memory"); } while (0)

#define TSD
#define TS(i)
#define TSW(k)
constexpr int SEQ = 2048, DM = 512, NH = 32, HD = 128, FH = 688, FI = 1376, ZLD = 768;
constexpr size_t WS_XN = 0;
constexpr size_t WS_Z1 = WS_XN + (size_t)SEQ * DM * 2;
constexpr size_t WS_Z2 = WS_Z1 + (size_t)SEQ * FH * 2;
constexpr size_t WS_Z2N = WS_Z2 + (size_t)SEQ * FH * 2;
constexpr size_t WS_H = WS_Z2N + (size_t)SEQ * ZLD * 2;
constexpr size_t WS_Q = 32u << 20;
constexpr size_t WS_K = WS_Q + (16u << 20);
constexpr size_t WS_V = WS_K + (16u << 20);
constexpr size_t WS_C = WS_V + (16u << 20);
constexpr size_t WS_P = WS_C + (16u << 20);
static_assert(WS_H + (size_t)SEQ * FH * 2 <= WS_Q, "ws map");

DI unsigned rfl(unsigned v) { return __builtin_amdgcn_readfirstlane(v); }
DI unsigned cvtpk(float lo, float hi) { f32x2 v = {lo, hi}; bf16x2 b = __builtin_convertvector(v, bf16x2); return __builtin_bit_cast(unsigned, b); }
DI float bflo(unsigned u) { return __uint_as_float(u << 16); }
DI float bfhi(unsigned u) { return __uint_as_float(u & 0xffff0000u); }
DI float rbf(float v) { return bflo(cvtpk(v, v) & 0xffffu); }
template <int S> DI u32x4 pack8(const f32x16& c) {
  u32x4 w = {cvtpk(c[8 * S + 0], c[8 * S + 1]), cvtpk(c[8 * S + 2], c[8 * S + 3]), cvtpk(c[8 * S + 4], c[8 * S + 5]), cvtpk(c[8 * S + 6], c[8 * S + 7])};
  return w;
}
DI rsrc_t mkrsrc(const void* p, unsigned bytes) { return __builtin_amdgcn_make_buffer_rsrc((void*)p, (short)0, (int)bytes, 0x00020000); }
template <int IMM> DI void dma16(rsrc_t r, unsigned voff, unsigned soff, unsigned m0v) {
  asm volatile("s_mov_b32 m0, %0\n\ts_nop 0\n\tbuffer_load_dwordx4 %1, %2, %3 offen offset:%4 lds" ::"s"(m0v), "v"(voff), "s"(r), "s"(soff), "i"(IMM) : "memory");
}
DI bf16x8 ldsr(LAS const char* p) { return *(LAS const bf16x8*)p; }
DI bf16x8 ldstr(LAS const char* p0, LAS const char* p1) {
  bf16x4 a = __builtin_amdgcn_ds_read_tr16_b64_v4bf16((LAS bf16x4*)p0);
  bf16x4 b = __builtin_amdgcn_ds_read_tr16_b64_v4bf16((LAS bf16x4*)p1);
  return __builtin_shufflevector(a, b, 0, 1, 2, 3, 4, 5, 6, 7);
}
DI float swap32_add(float v) { auto r = __builtin_amdgcn_permlane32_swap(__float_as_uint(v), __float_as_uint(v), false, false); return __uint_as_float(r[0]) + __uint_as_float(r[1]); }
DI float swap32_max(float v) { auto r = __builtin_amdgcn_permlane32_swap(__float_as_uint(v), __float_as_uint(v), false, false); return fmaxf(__uint_as_float(r[0]), __uint_as_float(r[1])); }
DI float gelu_t(float x) {
  const float u = x * (2.302208198f + 0.1029432397f * x * x);
  return x * __builtin_amdgcn_rcpf(1.f + __builtin_amdgcn_exp2f(-u));
}

constexpr int K0_LDS = 98304 + 4096;
__global__ __launch_bounds__(512, 2) void k0_ln_z(const u16* __restrict__ x, const u16* __restrict__ w_in, char* __restrict__ ws) {
  extern __shared__ __attribute__((aligned(16))) char lds_[];
  LAS char* lds = (LAS char*)lds_;
  const int tid = threadIdx.x, lane = tid & 63, wid = rfl(tid >> 6), r = lane & 31, h = lane >> 5;
  const int rbk = blockIdx.x / 15, cg = blockIdx.x % 15, wr = wid & 3, kh = wid >> 2;
  const int rb32 = rbk * 4 + wr;
  TSD; TS(0);
  u32x4 xa[16];
  {
    const char* xp = (const char*)x + (size_t)(rb32 * 32 + r) * 1024 + kh * 512 + h * 16;
#pragma unroll
    for (int i = 0; i < 16; ++i) asm volatile("global_load_dwordx4 %0, %1, off offset:%2" : "=v"(xa[i]) : "v"(xp), "i"(i * 32) : "memory");
  }
  {
    const rsrc_t rw = mkrsrc(w_in, DM * FI * 2);
    unsigned vo[3];
#pragma unroll
    for (int u = 0; u < 3; ++u) { const int i = 64 * u + lane; vo[u] = (i / 12) * (FI * 2) + (i % 12) * 16; }
    const unsigned l0 = (unsigned)(size_t)lds;
#pragma unroll
    for (int mm = 0; mm < 4; ++mm) {
      const unsigned m = wid + 8 * mm;
#pragma unroll
      for (int u = 0; u < 3; ++u) dma16<0>(rw, vo[u], m * 16 * (FI * 2) + cg * 192, l0 + (m * 3 + u) * 1024);
    }
  }
  TS(1);
  WAITV(12); SB();
  TS(2);
  LAS float* st = (LAS float*)(lds + 98304);
  float d[128];
  float s = 0.f;
#pragma unroll
  for (int i = 0; i < 16; ++i)
#pragma unroll
    for (int e = 0; e < 4; ++e) { d[8 * i + 2 * e] = bflo(xa[i][e]); d[8 * i + 2 * e + 1] = bfhi(xa[i][e]); s += d[8 * i + 2 * e] + d[8 * i + 2 * e + 1]; }
  s = swap32_add(s);
  if (h == 0) st[wid * 32 + r] = s;
  WAITL0(); BAR();
  const float mean = (st[wr * 32 + r] + st[(wr + 4) * 32 + r]) * (1.f / 512.f);
  const float mb = rbf(mean);
  float q = 0.f;
#pragma unroll
  for (int i = 0; i < 128; ++i) { d[i] -= mb; q = fmaf(d[i], d[i], q); }
  q = swap32_add(q);
  if (h == 0) st[256 + wid * 32 + r] = q;
  WAITL0(); BAR();
  const float dm = mean - mb;
  const float var = fmaxf((st[256 + wr * 32 + r] + st[256 + (wr + 4) * 32 + r]) * (1.f / 512.f) - dm * dm, 0.f);
  const float rs = 1.f / rbf(sqrtf(rbf(rbf(var) + 1e-5f)));
#pragma unroll
  for (int i = 0; i < 16; ++i)
#pragma unroll
    for (int e = 0; e < 4; ++e) xa[i][e] = cvtpk(d[8 * i + 2 * e] * rs, d[8 * i + 2 * e + 1] * rs);
  if (cg == 0) {
    u32x4* xo = (u32x4*)(ws + WS_XN + ((size_t)rb32 * 32 + kh * 16) * 1024 + lane * 16);
#pragma unroll
    for (int i = 0; i < 16; ++i) xo[i * 64] = xa[i];
  }
  TS(3);
  WAITV(0); BAR(); SB();
  TS(4);
  f32x16 acc[3] = {};
  {
    const int g = lane >> 4, q4 = (lane >> 2) & 3, p = lane & 3;
    LAS const char* bp = lds + (kh * 256 + 8 * h + q4) * 192 + (g & 1) * 32 + p * 8;
#pragma unroll
    for (int ks = 0; ks < 16; ++ks)
#pragma unroll
      for (int nb = 0; nb < 3; ++nb) {
        const bf16x8 wf = ldstr(bp + ks * 16 * 192 + nb * 64, bp + ks * 16 * 192 + 4 * 192 + nb * 64);
        acc[nb] = MFMA(wf, __builtin_bit_cast(bf16x8, xa[ks]), acc[nb]);
      }
  }
  TS(5);
  BAR();
  {
    LAS f32x4* ex = (LAS f32x4*)lds;
#pragma unroll
    for (int nb = 0; nb < 3; ++nb)
#pragma unroll
      for (int e = 0; e < 2; ++e) {
        f32x4 v;
#pragma unroll
        for (int c = 0; c < 4; ++c) v[c] = kh ? acc[nb][4 * e + c] : acc[nb][8 + 4 * e + c];
        ex[((wid * 3 + nb) * 2 + e) * 64 + lane] = v;
      }
    WAITL0(); BAR();
    const int pw = wid ^ 4;
#pragma unroll
    for (int nb = 0; nb < 3; ++nb) {
      float o[8];
#pragma unroll
      for (int e = 0; e < 2; ++e) {
        const f32x4 v = ex[((pw * 3 + nb) * 2 + e) * 64 + lane];
#pragma unroll
        for (int c = 0; c < 4; ++c) o[4 * e + c] = gelu_t(v[c] + (kh ? acc[nb][8 + 4 * e + c] : acc[nb][4 * e + c]));
      }
      const u32x4 w = {cvtpk(o[0], o[1]), cvtpk(o[2], o[3]), cvtpk(o[4], o[5]), cvtpk(o[6], o[7])};
      const int k16 = 2 * (3 * cg + nb) + kh;
      if (k16 < 43) *(u32x4*)(ws + WS_Z1 + ((size_t)rb32 * 43 + k16) * 1024 + lane * 16) = w;
      else if (k16 < 86) {
        char* zp = ws + WS_Z2 + (size_t)(rb32 * 32 + r) * (FH * 2) + (k16 - 43) * 32 + h * 8;
        *(u32x2*)zp = (u32x2){w[0], w[1]};
        *(u32x2*)(zp + 16) = (u32x2){w[2], w[3]};
      }
    }
  }
  TS(6); WAITV(0); TS(7); TSW(0);
}

constexpr int K1_LDS = 131072;
constexpr float QSCALE = 0.08838834764831845f * 1.4426950408889634f;
struct K1C {
  rsrc_t rA, rW0, rW1, rXn, rWq;
  unsigned voA[2], voB, voX, ldsb, m0A, m0B, soA, soB, soX;
  LAS const char* aR[4]; LAS const char* aX; LAS const char* bR;
};
template <int ST, int I, bool QN> DI void k1_dma(const K1C& c, int t) {
  if constexpr (!QN) {
    if constexpr (I < 4) dma16<0>(c.rA, c.voA[I & 1], c.soA + t * 128 + I * 8192, c.m0A + ST * 32768 + I * 1024);
    else if constexpr (I < 6) dma16<0>(c.rW0, c.voB, c.soB + t * 524288 + (I - 4) * 32768, c.m0B + ST * 32768 + (I - 4) * 1024);
    else dma16<0>(c.rW1, c.voB, c.soB + t * 524288 + (I - 6) * 32768, c.m0B + ST * 32768 + 16384 + (I - 6) * 1024);
  } else {
    if constexpr (I < 4) dma16<I * 1024>(c.rXn, c.voX, c.soX + (t - 8) * 4096, c.m0A + ST * 32768);
    else if constexpr (I < 6) dma16<0>(c.rWq, c.voB, c.soB + (t - 8) * 524288 + (I - 4) * 32768, c.m0B + ST * 32768 + (I - 4) * 1024);
  }
}
struct K1F { bf16x8 a[4], w0, w1; };
template <int ST, int KS, bool Q> DI void k1_rd(const K1C& c, K1F& f) {
#pragma unroll
  for (int i = 0; i < 4; ++i) f.a[i] = Q ? ldsr(c.aX + ST * 32768 + i * 4096 + KS * 1024) : ldsr(c.aR[KS] + ST * 32768 + i * 4096);
  f.w0 = ldstr(c.bR + ST * 32768 + KS * 4096, c.bR + ST * 32768 + KS * 4096 + 1024);
  if constexpr (!Q) f.w1 = ldstr(c.bR + ST * 32768 + 16384 + KS * 4096, c.bR + ST * 32768 + 16384 + KS * 4096 + 1024);
}
template <bool Q> DI void k1_mm(const K1F& f, f32x16 (&a0)[4], f32x16 (&a1)[4], int lo, int hi) {
#pragma unroll
  for (int i = 0; i < 4; ++i) if (i >= lo && i < hi) { a0[i] = MFMA(f.w0, f.a[i], a0[i]); if constexpr (!Q) a1[i] = MFMA(f.a[i], f.w1, a1[i]); }
}
template <int ST, bool Q, bool QN1, bool QN2, bool DM2, bool RD1, int VW> DI void k1_tile(const K1C& c, int t, K1F& f0, K1F& f1, f32x16 (&a0)[4], f32x16 (&a1)[4]) {
  k1_rd<ST, 1, Q>(c, f1); SB(); k1_mm<Q>(f0, a0, a1, 0, 4); SB();
  k1_rd<ST, 2, Q>(c, f0); SB(); k1_mm<Q>(f1, a0, a1, 0, 4); SB();
  k1_rd<ST, 3, Q>(c, f1); SB(); k1_mm<Q>(f0, a0, a1, 0, 4); SB();
  asm volatile("s_waitcnt vmcnt(%0) lgkmcnt(0)" ::"i"(VW) : "memory"); BAR(); SB();
  if constexpr (RD1) k1_rd<ST ^ 1, 0, QN1>(c, f0);
  SB();
  k1_mm<Q>(f1, a0, a1, 0, 1); SB(); if constexpr (DM2) { k1_dma<ST, 0, QN2>(c, t + 2); k1_dma<ST, 1, QN2>(c, t + 2); } SB();
  k1_mm<Q>(f1, a0, a1, 1, 2); SB(); if constexpr (DM2) { k1_dma<ST, 2, QN2>(c, t + 2); k1_dma<ST, 3, QN2>(c, t + 2); } SB();
  k1_mm<Q>(f1, a0, a1, 2, 3); SB(); if constexpr (DM2) { k1_dma<ST, 4, QN2>(c, t + 2); k1_dma<ST, 5, QN2>(c, t + 2); } SB();
  k1_mm<Q>(f1, a0, a1, 3, 4); SB(); if constexpr (DM2) { k1_dma<ST, 6, QN2>(c, t + 2); k1_dma<ST, 7, QN2>(c, t + 2); } SB();
}
__global__ __launch_bounds__(512, 2) void k1_qkv(const u16* __restrict__ ckv, const u16* __restrict__ wq, const u16* __restrict__ wk, const u16* __restrict__ wv, char* __restrict__ ws) {
  extern __shared__ __attribute__((aligned(16))) char lds_[];
  LAS char* lds = (LAS char*)lds_;
  const int tid = threadIdx.x, lane = tid & 63, wid = rfl(tid >> 6), r = lane & 31, h = lane >> 5;
  const int bid = blockIdx.x, idx = bid >> 3, head = (bid & 7) * 4 + (idx >> 3), rb = idx & 7;
  const int wm = wid >> 2, wn = wid & 3;
  K1C c;
  TSD; TS(0);
  c.rA = mkrsrc(ckv, SEQ * DM * 2); c.rW0 = mkrsrc(wk, DM * 4096 * 2); c.rW1 = mkrsrc(wv, DM * 4096 * 2); c.rWq = mkrsrc(wq, DM * 4096 * 2);
  c.rXn = mkrsrc(ws + WS_XN, SEQ * DM * 2);
  c.ldsb = (unsigned)(size_t)lds;
#pragma unroll
  for (int p = 0; p < 2; ++p) c.voA[p] = (lane >> 3) * 1024 + (((lane & 7) ^ (4 * p + (lane >> 4))) * 16);
  c.voB = (lane >> 4) * 8192 + (((lane & 15) ^ ((lane >> 4) << 2)) * 16);
  c.voX = lane * 16;
  c.soA = (rb * 256 + wid * 32) * 1024; c.soB = wid * 8 * 8192 + head * 256; c.soX = (rb * 8 + wid) * 32768;
  c.m0A = c.ldsb + wid * 4096; c.m0B = c.ldsb + 65536 + wid * 2048;
  {
    const int tt = h ^ ((r >> 1) & 7);
#pragma unroll
    for (int ks = 0; ks < 4; ++ks) c.aR[ks] = lds + (wm * 128 + r) * 128 + ((tt ^ (2 * ks)) * 16);
    c.aX = lds + wm * 16384 + lane * 16;
    const int g = lane >> 4, q4 = (lane >> 2) & 3, p = lane & 3;
    c.bR = lds + 65536 + (8 * h + q4) * 256 + ((wn ^ q4) * 64) + (g & 1) * 32 + p * 8;
  }
  k1_dma<0, 0, false>(c, 0); k1_dma<0, 1, false>(c, 0); k1_dma<0, 2, false>(c, 0); k1_dma<0, 3, false>(c, 0);
  k1_dma<0, 4, false>(c, 0); k1_dma<0, 5, false>(c, 0); k1_dma<0, 6, false>(c, 0); k1_dma<0, 7, false>(c, 0);
  k1_dma<1, 0, false>(c, 1); k1_dma<1, 1, false>(c, 1); k1_dma<1, 2, false>(c, 1); k1_dma<1, 3, false>(c, 1);
  k1_dma<1, 4, false>(c, 1); k1_dma<1, 5, false>(c, 1); k1_dma<1, 6, false>(c, 1); k1_dma<1, 7, false>(c, 1);
  {
    const int row = bid * 8 + wid;
    const u32x4* zp = (const u32x4*)(ws + WS_Z2 + (size_t)row * (FH * 2));
    const bool act = lane < 43;
    u32x4 v0 = {}, v1 = {};
    if (act) { v0 = zp[lane]; v1 = zp[lane + 43]; }
    float d[16]; float s = 0.f;
#pragma unroll
    for (int e = 0; e < 4; ++e) { d[2 * e] = bflo(v0[e]); d[2 * e + 1] = bfhi(v0[e]); d[8 + 2 * e] = bflo(v1[e]); d[9 + 2 * e] = bfhi(v1[e]); }
#pragma unroll
    for (int e = 0; e < 16; ++e) s += d[e];
#pragma unroll
    for (int o = 1; o < 64; o <<= 1) s += __shfl_xor(s, o);
    const float mean = s * (1.f / FH), mb = rbf(mean);
    float q = 0.f;
#pragma unroll
    for (int e = 0; e < 16; ++e) { d[e] -= mb; q = fmaf(d[e], d[e], q); }
    if (!act) q = 0.f;
#pragma unroll
    for (int o = 1; o < 64; o <<= 1) q += __shfl_xor(q, o);
    const float dm = mean - mb, var = fmaxf(q * (1.f / FH) - dm * dm, 0.f);
    const float rs = 1.f / rbf(sqrtf(rbf(rbf(var) + 1e-5f)));
    const int prow = (row & ~12) | ((row & 4) << 1) | ((row & 8) >> 1);
    u32x4* op = (u32x4*)(ws + WS_Z2N + (size_t)prow * (ZLD * 2));
    if (act) {
#pragma unroll
      for (int e = 0; e < 4; ++e) { v0[e] = cvtpk(d[2 * e] * rs, d[2 * e + 1] * rs); v1[e] = cvtpk(d[8 + 2 * e] * rs, d[9 + 2 * e] * rs); }
      op[lane] = v0; op[lane + 43] = v1;
    } else if (lane < 53) op[lane + 43] = (u32x4){0u, 0u, 0u, 0u};
  }
  f32x16 a0[4] = {}, a1[4] = {};
  K1F f0, f1;
  TS(1);
  WAITV(8); BAR(); SB();
  TS(2);
  k1_rd<0, 0, false>(c, f0); SB();
  for (int t = 0; t < 6; t += 2) {
    k1_tile<0, false, false, false, true, true, 0>(c, t, f0, f1, a0, a1);
    k1_tile<1, false, false, false, true, true, 0>(c, t + 1, f0, f1, a0, a1);
  }
  k1_tile<0, false, false, true, true, true, 0>(c, 6, f0, f1, a0, a1);
  k1_tile<1, false, true, true, true, true, 0>(c, 7, f0, f1, a0, a1);
  TS(3);
  {
    const size_t o = ((size_t)(head * 64 + rb * 8 + wm * 4) * 8 + 2 * wn) * 1024 + lane * 16;
#pragma unroll
    for (int i = 0; i < 4; ++i) {
      *(u32x4*)(ws + WS_K + o + i * 8192) = pack8<0>(a0[i]); *(u32x4*)(ws + WS_K + o + i * 8192 + 1024) = pack8<1>(a0[i]);
      *(u32x4*)(ws + WS_V + o + i * 8192) = pack8<0>(a1[i]); *(u32x4*)(ws + WS_V + o + i * 8192 + 1024) = pack8<1>(a1[i]);
    }
#pragma unroll
    for (int i = 0; i < 4; ++i) a0[i] = (f32x16){};
  }
  SB();
  TS(4);
  k1_tile<0, true, true, true, true, true, 16>(c, 8, f0, f1, a0, a1);
  k1_tile<1, true, true, true, true, true, 0>(c, 9, f0, f1, a0, a1);
  for (int t = 10; t < 14; t += 2) {
    k1_tile<0, true, true, true, true, true, 0>(c, t, f0, f1, a0, a1);
    k1_tile<1, true, true, true, true, true, 0>(c, t + 1, f0, f1, a0, a1);
  }
  k1_tile<0, true, true, true, false, true, 0>(c, 14, f0, f1, a0, a1);
  k1_tile<1, true, true, true, false, false, 0>(c, 15, f0, f1, a0, a1);
  TS(5);
  {
    const size_t o = ((size_t)(head * 64 + rb * 8 + wm * 4) * 8 + 2 * wn) * 1024 + lane * 16;
#pragma unroll
    for (int i = 0; i < 4; ++i) {
      a0[i] *= QSCALE;
      *(u32x4*)(ws + WS_Q + o + i * 8192) = pack8<0>(a0[i]); *(u32x4*)(ws + WS_Q + o + i * 8192 + 1024) = pack8<1>(a0[i]);
    }
  }
  TS(6); WAITV(0); TS(7); TSW(1);
}

constexpr int K2_LDS = 131072 + 4096;
constexpr float THR = 32.f;
struct K2C {
  rsrc_t rK, rV, rC;
  unsigned vo, m0g, sog;
  LAS const char* rd;
  int n1, g, qrow;
  int rowA, rowB;
};
DI int k2_tile(const K2C& c, int n) { n = n > 16 ? 16 : n; return n < c.n1 ? n : 2 * n - c.n1 + c.g; }
DI unsigned k2_tile_off(const K2C& c, int n) { return c.sog + (unsigned)k2_tile(c, n) * 16384u; }
template <int IMM> DI void dma16n(rsrc_t r, unsigned voff, unsigned soff) {
  asm volatile("buffer_load_dwordx4 %0, %1, %2 offen offset:%3 lds" ::"v"(voff), "s"(r), "s"(soff), "i"(IMM) : "memory");
}
template <int OFF> DI void k2_dma4(rsrc_t r, const K2C& c, unsigned so) {
  dma16<0>(r, c.vo, so, c.m0g + OFF); dma16n<1024>(r, c.vo, so); dma16n<2048>(r, c.vo, so); dma16n<3072>(r, c.vo, so);
}
DI void k2_mask(f32x16 (&s)[2], int dq) {
#pragma unroll
  for (int kb = 0; kb < 2; ++kb)
#pragma unroll
    for (int e = 0; e < 16; ++e) { const int cc = 32 * kb + (e & 3) + 8 * (e >> 2); if (cc > dq) s[kb][e] = -__builtin_inff(); }
}
DI float max3(float a, float b, float c) { float d; asm("v_max3_f32 %0, %1, %2, %3" : "=v"(d) : "v"(a), "v"(b), "v"(c)); return d; }
DI float k2_max(const f32x16 (&s)[2]) {
  float m0 = fmaxf(s[0][0], s[0][1]), m1 = fmaxf(s[1][0], s[1][1]);
#pragma unroll
  for (int e = 2; e < 16; e += 2) { m0 = fmaxf(fmaxf(m0, s[0][e]), s[0][e + 1]); m1 = fmaxf(fmaxf(m1, s[1][e]), s[1][e + 1]); }
  return fmaxf(m0, m1);
}
DI void addf(float& a, float b) { asm("v_add_f32 %0, %0, %1" : "+v"(a) : "v"(b)); }
DI void k2_loadq(u32x4 (&q)[8], const char* p, unsigned vo, int a, int b) {
  asm volatile("s_cmp_lg_u32 %11, %12\n\ts_cbranch_scc1 .Lq%=\n\t"
               "global_load_dwordx4 %0, %8, %9\n\tglobal_load_dwordx4 %1, %8, %9 offset:1024\n\tglobal_load_dwordx4 %2, %8, %9 offset:2048\n\tglobal_load_dwordx4 %3, %8, %9 offset:3072\n\t"
               "global_load_dwordx4 %4, %8, %10\n\tglobal_load_dwordx4 %5, %8, %10 offset:1024\n\tglobal_load_dwordx4 %6, %8, %10 offset:2048\n\tglobal_load_dwordx4 %7, %8, %10 offset:3072\n"
               ".Lq%=:" : "+v"(q[0]), "+v"(q[1]), "+v"(q[2]), "+v"(q[3]), "+v"(q[4]), "+v"(q[5]), "+v"(q[6]), "+v"(q[7])
               : "v"(vo), "s"(p), "s"(p + 4096), "s"(a), "s"(b) : "scc", "memory");
}
DI void bst16(u32x4 v, rsrc_t r, unsigned vo, unsigned so) { __builtin_amdgcn_raw_buffer_store_b128(v, r, vo, so, 0); }
struct K2S { f32x16 o[4], negm; u32x4 q[8]; bf16x8 f[3]; float m, l; };

DI void k2_rescale(K2S& S, f32x16 (&sn)[2], float lmax, bool first) {
  float dl = swap32_max(lmax);
  if (first) { dl = dl == -__builtin_inff() ? 0.f : dl; S.m = dl; }
  else {
    dl = fmaxf(dl, 0.f); S.m += dl;
    const float f = __builtin_amdgcn_exp2f(-dl);
    S.l *= f;
#pragma unroll
    for (int db = 0; db < 4; ++db) S.o[db] *= f;
  }
#pragma unroll
  for (int kb = 0; kb < 2; ++kb) sn[kb] -= dl;
#pragma unroll
  for (int e = 0; e < 16; ++e) S.negm[e] = -S.m;
}
DI void k2_finishB(const K2C& c, K2S& S, unsigned ctx) {
  const float inv = __builtin_amdgcn_rcpf(swap32_add(S.l));
#pragma unroll
  for (int db = 0; db < 4; ++db) {
    const f32x16 res = S.o[db] * inv;
    bst16(pack8<0>(res), c.rC, c.vo, ctx + db * 2048); bst16(pack8<1>(res), c.rC, c.vo, ctx + db * 2048 + 1024);
    S.o[db] = (f32x16){};
  }
  S.l = 0.f;
}
DI void k2_finishA(const K2C& c, K2S& S, LAS char* lds, int wid, unsigned ctx) {
  const int lane = c.vo >> 4;
  const int g = wid >> 2, pw = wid ^ 4;
  LAS f32x4* ex = (LAS f32x4*)lds;
  LAS f32x2* es = (LAS f32x2*)(lds + 131072);
  const float lt = swap32_add(S.l);
  const float me = lt > 0.f ? S.m : -1e30f;
  es[wid * 64 + lane] = (f32x2){me, lt};
#pragma unroll
  for (int rd = 0; rd < 2; ++rd) {
    const f32x16 snd = g ? S.o[rd] : S.o[2 + rd];
#pragma unroll
    for (int e = 0; e < 4; ++e) ex[(wid * 8 + rd * 4 + e) * 64 + lane] = (f32x4){snd[4 * e], snd[4 * e + 1], snd[4 * e + 2], snd[4 * e + 3]};
  }
  WAITL0(); BAR();
  const f32x2 ps = es[pw * 64 + lane];
  const float mm = fmaxf(me, ps[0]);
  float w0 = __builtin_amdgcn_exp2f(me - mm), w1 = __builtin_amdgcn_exp2f(ps[0] - mm);
  const float inv = __builtin_amdgcn_rcpf(w0 * lt + w1 * ps[1]);
  w0 *= inv; w1 *= inv;
#pragma unroll
  for (int rd = 0; rd < 2; ++rd) {
    const f32x16 own = g ? S.o[2 + rd] : S.o[rd];
    f32x16 res;
#pragma unroll
    for (int e = 0; e < 4; ++e) {
      const f32x4 v = ex[(pw * 8 + rd * 4 + e) * 64 + lane];
#pragma unroll
      for (int c = 0; c < 4; ++c) res[4 * e + c] = own[4 * e + c] * w0 + v[c] * w1;
    }
    const unsigned p = ctx + (2 * g + rd) * 2048;
    bst16(pack8<0>(res), c.rC, c.vo, p); bst16(pack8<1>(res), c.rC, c.vo, p + 1024);
  }
}
template <int PAR> DI bf16x8 k2_frag(const K2C& c, int i) {
  if (i < 16) return ldsr(c.rd + (PAR ^ 1) * 16384 + i * 1024);
  if (i >= 32) return ldsr(c.rd + PAR * 16384 + (i - 32) * 1024);
  const int ks = (i - 16) >> 2, db = i & 3;
  return ldsr(c.rd + 32768 + PAR * 16384 + (ks >> 1) * 8192 + db * 2048 + (ks & 1) * 1024);
}
template <int PAR, bool NEXT> DI void k2_step(const K2C& c, K2S& S, int n, f32x16 (&sc)[2], f32x16 (&sn)[2], const char* qnext, unsigned ctxB) {
  bf16x8 F[35];
  F[0] = S.f[0]; F[1] = S.f[1]; F[2] = S.f[2];
  u32x4 p[4];
  float l0 = S.l, l1 = 0.f, lmax = 0.f;
  unsigned soK = 0, soV = 0;
  if constexpr (NEXT) { soV = k2_tile_off(c, n + 1); soK = k2_tile_off(c, n + 3); }
#pragma unroll
  for (int s = 0; s < 32; ++s) {
    if (s == 29) { asm volatile("s_waitcnt vmcnt(0) lgkmcnt(0)" ::: "memory"); BAR(); SB(); }
    if (s < 16) { if constexpr (NEXT) sn[s >> 3] = MFMA(F[s], __builtin_bit_cast(bf16x8, S.q[s & 7]), (s & 7) == 0 ? S.negm : sn[s >> 3]); }
    else S.o[s & 3] = MFMA(F[s], __builtin_bit_cast(bf16x8, p[(s - 16) >> 2]), S.o[s & 3]);
    SB();
    if (s == 0 && NEXT) F[3] = k2_frag<PAR>(c, 3);
    if (s <= 27 && (NEXT || s >= 12)) F[s + 4] = k2_frag<PAR>(c, s + 4);
    if (s >= 29 && NEXT) F[s + 3] = k2_frag<PAR>(c, s + 3);
    const int e0 = s < 5 ? 2 * s : s + 5, e1 = s < 5 ? 2 * s + 2 : s + 6;
    const int a0 = s < 6 ? 2 * s - 2 : s + 4, a1 = s < 6 ? 2 * s : s + 5;
    if (s <= 26) {
#pragma unroll
      for (int e = e0; e < e1; ++e) sc[e >> 4][e & 15] = __builtin_amdgcn_exp2f(sc[e >> 4][e & 15]);
    }
    if (s >= 1 && s <= 27) {
#pragma unroll
      for (int e = a0; e < a1; ++e) {
        if (e & 1) { addf(l1, sc[e >> 4][e & 15]); p[e >> 3][(e >> 1) & 3] = cvtpk(sc[e >> 4][(e & 15) - 1], sc[e >> 4][e & 15]); }
        else addf(l0, sc[e >> 4][e & 15]);
      }
    }
    if constexpr (NEXT) {
      if (s == 10) lmax = max3(sn[0][0], sn[0][1], sn[0][1]);
      if (s > 10 && s <= 17) lmax = max3(lmax, sn[0][2 * (s - 10)], sn[0][2 * (s - 10) + 1]);
      if (s >= 19 && s <= 26) lmax = max3(lmax, sn[1][2 * (s - 19)], sn[1][2 * (s - 19) + 1]);
      if (s == 1) dma16<0>(c.rV, c.vo, soV, c.m0g + 32768 + (PAR ^ 1) * 16384);
      if (s == 2) dma16n<1024>(c.rV, c.vo, soV);
      if (s == 3) dma16n<2048>(c.rV, c.vo, soV);
      if (s == 4) dma16n<3072>(c.rV, c.vo, soV);
      if (s == 16) k2_loadq(S.q, qnext, c.vo, n + 2, c.g ? c.n1 : -1);
      if (s == 29) { dma16<0>(c.rK, c.vo, soK, c.m0g + (PAR ^ 1) * 16384); }
      if (s == 30) { dma16n<1024>(c.rK, c.vo, soK); }
      if (s == 31) { dma16n<2048>(c.rK, c.vo, soK); dma16n<3072>(c.rK, c.vo, soK); }
    }
    if (s == 28) { S.l = l0 + l1; asm volatile("" : "+v"(S.l), "+v"(lmax)); }
    SB();
  }
  if constexpr (NEXT) {
    S.f[0] = F[32]; S.f[1] = F[33]; S.f[2] = F[34];
    const bool first = c.g && n + 1 == c.n1;
    const int kb = k2_tile(c, n + 1) * 64, rw = n + 1 < c.n1 ? c.rowB : c.rowA;
    if (kb + 63 > rw) { k2_mask(sn, rw + c.qrow - kb); lmax = k2_max(sn); }
    if (first || __any(lmax > THR)) k2_rescale(S, sn, lmax, first);
    if (first) k2_finishB(c, S, ctxB);
    if (c.g && n + 2 == c.n1) {
#pragma unroll
      for (int e = 0; e < 16; ++e) S.negm[e] = 0.f;
    }
    SB();
  }
}
__global__ __launch_bounds__(512, 2) void k2_attn(char* __restrict__ ws) {
  extern __shared__ __attribute__((aligned(16))) char lds_[];
  LAS char* lds = (LAS char*)lds_;
  const int tid = threadIdx.x, lane = tid & 63, wid = rfl(tid >> 6), r = lane & 31, h = lane >> 5;
  const int bid = blockIdx.x, idx = bid >> 3, head = (bid & 7) * 4 + (idx >> 3), j = idx & 7;
  const int g = wid >> 2, wq = wid & 3;
  TSD; TS(0);
  K2C c;
  c.rK = mkrsrc(ws + WS_K + (size_t)head * 524288, 524288); c.rV = mkrsrc(ws + WS_V + (size_t)head * 524288, 524288);
  c.vo = lane * 16; c.m0g = (unsigned)(size_t)lds + g * 65536 + wq * 4096; c.sog = wq * 4096;
  c.rd = lds + g * 65536 + lane * 16; c.n1 = 2 * j + 2; c.g = g; c.qrow = r - 4 * h;
  const int rbA = (15 - j) * 4 + wq, rbB = g ? j * 4 + wq : rbA;
  c.rowA = rbA * 32; c.rowB = rbB * 32;
  const char* qA = ws + WS_Q + (size_t)(head * 64 + rbA) * 8192;
  const char* qB = ws + WS_Q + (size_t)(head * 64 + rbB) * 8192;
  c.rC = mkrsrc(ws + WS_C, 16u << 20);
  const unsigned ctxA = (rbA * 32 + head) * 8192, ctxB = (rbB * 32 + head) * 8192;
  K2S S;
#pragma unroll
  for (int i = 0; i < 8; ++i) S.q[i] = (u32x4){};
  k2_loadq(S.q, qB, c.vo, 0, 0);
  k2_dma4<0>(c.rK, c, k2_tile_off(c, 0));
  k2_dma4<16384>(c.rK, c, k2_tile_off(c, 1));
  k2_dma4<32768>(c.rV, c, k2_tile_off(c, 0));
#pragma unroll
  for (int db = 0; db < 4; ++db) S.o[db] = (f32x16){};
  S.negm = (f32x16){}; S.m = 0.f; S.l = 0.f;
  f32x16 sa[2], sb[2];
  TS(1);
  WAITV(8); BAR(); SB();
  TS(2);
#pragma unroll
  for (int s = 0; s < 16; ++s) sa[s >> 3] = MFMA(k2_frag<1>(c, s), __builtin_bit_cast(bf16x8, S.q[s & 7]), (s & 7) == 0 ? S.negm : sa[s >> 3]);
  if (63 > c.rowB) k2_mask(sa, c.rowB + c.qrow);
  k2_rescale(S, sa, k2_max(sa), true);
  SB();
  asm volatile("s_waitcnt vmcnt(0) lgkmcnt(0)" ::: "memory"); BAR(); SB();
  k2_dma4<0>(c.rK, c, k2_tile_off(c, 2));
#pragma unroll
  for (int i = 0; i < 3; ++i) S.f[i] = k2_frag<0>(c, i);
  TS(3);
  for (int n = 0; n < 16; n += 2) {
    k2_step<0, true>(c, S, n, sa, sb, qA, ctxB);
    k2_step<1, true>(c, S, n + 1, sb, sa, qA, ctxB);
  }
  TS(4);
  k2_step<0, false>(c, S, 16, sa, sb, qA, ctxB);
  TS(5);
  k2_finishA(c, S, lds, wid, ctxA);
  TS(6); WAITV(0); TS(7); TSW(2);
}

constexpr int K3_LDS = 131072;
__global__ __launch_bounds__(512, 2) void k3_gemm(const u16* __restrict__ wo, const u16* __restrict__ wsp, const u16* __restrict__ bsp, char* __restrict__ ws) {
  extern __shared__ __attribute__((aligned(16))) char lds_[];
  LAS char* lds = (LAS char*)lds_;
  const int tid = threadIdx.x, lane = tid & 63, wid = rfl(tid >> 6), r = lane & 31, h = lane >> 5;
  const int bid = blockIdx.x, xcd = bid & 7, idx = bid >> 3;
  const bool isA = idx < 16;
  const int wm = wid >> 2, wn = wid & 3;
  int rb, nb, sp = 0;
  if (isA) { sp = xcd & 1; rb = (xcd >> 1) * 4 + (idx >> 2); nb = idx & 3; } else { rb = xcd * 2 + (idx - 16) / 6; nb = (idx - 16) % 6; }
  rsrc_t rA, rB; unsigned voA[2], soA, stA, voB, soB, stB, ldb;
  LAS const char* aR[4];
  if (isA) {
    rA = mkrsrc(ws + WS_C, 16u << 20); rB = mkrsrc(wo, 4096 * DM * 2); ldb = 1024;
    voA[0] = lane * 16; voA[1] = lane * 16 + 1024; soA = ((rb * 4 + (wid >> 1)) * 64 + sp * 32) * 4096 + (wid & 1) * 2048; stA = 4096;
    soB = sp * 2048 * 1024 + nb * 256;
#pragma unroll
    for (int ks = 0; ks < 4; ++ks) aR[ks] = lds + wm * 8192 + ks * 1024 + lane * 16;
  } else {
    rA = mkrsrc(wsp, SEQ * SEQ * 2); rB = mkrsrc(ws + WS_Z2N, SEQ * ZLD * 2); ldb = ZLD * 2;
#pragma unroll
    for (int i = 0; i < 2; ++i) voA[i] = (8 * i + (lane >> 3)) * 4096 + (((lane & 7) ^ (4 * i + (lane >> 4))) * 16);
    soA = (rb * 128 + wid * 16) * 4096; stA = 128;
    soB = nb * 256;
    const int tt = h ^ ((r >> 1) & 7);
#pragma unroll
    for (int ks = 0; ks < 4; ++ks) aR[ks] = lds + (wm * 64 + r) * 128 + ((tt ^ (2 * ks)) * 16);
  }
  voB = (lane >> 4) * ldb + (((lane & 15) ^ ((lane >> 4) << 2)) * 16);
  soB += wid * 8 * ldb; stB = 64 * ldb;
  const unsigned sB1 = 4 * ldb, m0 = (unsigned)(size_t)lds + wid * 2048;
  const int g = lane >> 4, q4 = (lane >> 2) & 3, p = lane & 3;
  LAS const char* bR = lds + 16384 + (4 * h + q4) * 256 + ((wn ^ q4) * 64) + (g & 1) * 32 + p * 8;
#define K3_DMA(ST, t) do { dma16<0>(rA, voA[0], soA + (t) * stA, m0 + (ST) * 32768); dma16<0>(rA, voA[1], soA + (t) * stA, m0 + (ST) * 32768 + 1024); \
    dma16<0>(rB, voB, soB + (t) * stB, m0 + (ST) * 32768 + 16384); dma16<0>(rB, voB, soB + (t) * stB + sB1, m0 + (ST) * 32768 + 16384 + 1024); } while (0)
#define K3_TILE(ST, t) do { WAITV(8); BAR(); SB(); { const int tn = (t) + 3 < 32 ? (t) + 3 : 31; K3_DMA(((ST) + 3) & 3, tn); } SB(); \
    _Pragma("unroll") for (int ks = 0; ks < 4; ++ks) { \
      LAS const char* ap = aR[ks] + ((ST) & 1) * 32768 + ((ST) >> 1) * 65536; LAS const char* bp = bR + ((ST) & 1) * 32768 + ((ST) >> 1) * 65536 + ks * 4096; \
      const bf16x8 a0 = ldsr(ap), a1 = ldsr(ap + 4096), bf = ldstr(bp, bp + 2048); \
      acc[0] = MFMA(bf, a0, acc[0]); acc[1] = MFMA(bf, a1, acc[1]); } SB(); } while (0)
  f32x16 acc[2] = {};
  TSD; TS(0);
  K3_DMA(0, 0); K3_DMA(1, 1); K3_DMA(2, 2);
  K3_TILE(0, 0); TS(1); K3_TILE(1, 1); K3_TILE(2, 2); K3_TILE(3, 3); TS(2);
  for (int t = 4; t < 32; t += 4) { K3_TILE(0, t); K3_TILE(1, t + 1); K3_TILE(2, t + 2); K3_TILE(3, t + 3); }
  TS(3);
  WAITV(0);
  if (isA) {
#pragma unroll
    for (int i = 0; i < 2; ++i) {
      f32x4* pp = (f32x4*)(ws + WS_P + ((size_t)((sp * 64 + rb * 4 + wm * 2 + i) * 16 + nb * 4 + wn) * 4) * 1024 + lane * 16);
#pragma unroll
      for (int e = 0; e < 4; ++e) pp[e * 64] = (f32x4){acc[i][4 * e], acc[i][4 * e + 1], acc[i][4 * e + 2], acc[i][4 * e + 3]};
    }
  } else {
#pragma unroll
    for (int i = 0; i < 2; ++i) {
      const int rb32 = rb * 4 + wm * 2 + i;
      const float b = bflo(bsp[rb32 * 32 + r]);
#pragma unroll
      for (int s = 0; s < 2; ++s) {
        const int k16 = 2 * (nb * 4 + wn) + s;
        if (k16 < 43) {
          const size_t o = ((size_t)rb32 * 43 + k16) * 1024 + lane * 16;
          const u32x4 z = *(const u32x4*)(ws + WS_Z1 + o);
          u32x4 w;
#pragma unroll
          for (int e = 0; e < 4; ++e) w[e] = cvtpk(bflo(z[e]) * (acc[i][8 * s + 2 * e] + b), bfhi(z[e]) * (acc[i][8 * s + 2 * e + 1] + b));
          *(u32x4*)(ws + WS_H + o) = w;
        }
      }
    }
  }
  TS(4); WAITV(0); TS(5); TSW(3);
}

constexpr int K4_LDS = 88064;
__global__ __launch_bounds__(256, 1) void k4_out(const u16* __restrict__ x, const u16* __restrict__ wout, char* __restrict__ ws, u16* __restrict__ out) {
  extern __shared__ __attribute__((aligned(16))) char lds_[];
  LAS char* lds = (LAS char*)lds_;
  const int tid = threadIdx.x, lane = tid & 63, wid = rfl(tid >> 6), r = lane & 31, h = lane >> 5;
  const int bid = blockIdx.x, rb = (bid & 7) * 4 + ((bid >> 3) & 3), cb = bid >> 5;
  const int wm = wid >> 1, wn = wid & 1, rb32 = rb * 2 + wm, nb32 = cb * 2 + wn;
  TSD; TS(0);
  {
    const rsrc_t rw = mkrsrc(wout, FH * DM * 2);
    const unsigned vo = (lane >> 3) * 1024 + (((lane & 7) ^ (((lane >> 4) & 1) << 2)) * 16), l0 = (unsigned)(size_t)lds;
    for (int pc = wid; pc < 86; pc += 4) dma16<0>(rw, vo, pc * 8192 + cb * 128, l0 + pc * 1024);
  }
  u32x4 hf[43];
  const u32x4* hp = (const u32x4*)(ws + WS_H + (size_t)rb32 * 43 * 1024 + lane * 16);
#pragma unroll
  for (int i = 0; i < 43; ++i) hf[i] = hp[i * 64];
  f32x16 acc;
  {
    const f32x4* p0 = (const f32x4*)(ws + WS_P + ((size_t)(rb32 * 16 + nb32) * 4) * 1024 + lane * 16);
    const f32x4* p1 = (const f32x4*)((const char*)p0 + (4u << 20));
    const char* xp = (const char*)x + (size_t)(rb32 * 32 + r) * 1024 + nb32 * 64 + h * 8;
#pragma unroll
    for (int e = 0; e < 4; ++e) {
      const f32x4 a = p0[e * 64], b = p1[e * 64]; const u32x2 xv = *(const u32x2*)(xp + e * 16);
      acc[4 * e] = a[0] + b[0] + bflo(xv[0]); acc[4 * e + 1] = a[1] + b[1] + bfhi(xv[0]); acc[4 * e + 2] = a[2] + b[2] + bflo(xv[1]); acc[4 * e + 3] = a[3] + b[3] + bfhi(xv[1]);
    }
  }
  TS(1);
  WAITV(0); BAR(); SB();
  TS(2);
  const int g = lane >> 4, q4 = (lane >> 2) & 3, p = lane & 3;
  LAS const char* bR = lds + (4 * h + q4) * 128 + ((wn ^ (q4 >> 1)) * 64) + (g & 1) * 32 + p * 8;
#pragma unroll
  for (int ks = 0; ks < 43; ++ks) {
    LAS const char* bp = bR + (ks & 15) * 2048 + (ks >> 4) * 32768;
    acc = MFMA(ldstr(bp, bp + 1024), __builtin_bit_cast(bf16x8, hf[ks]), acc);
  }
  TS(3);
  char* op = (char*)out + (size_t)(rb32 * 32 + r) * 1024 + nb32 * 64 + h * 8;
#pragma unroll
  for (int e = 0; e < 4; ++e) *(u32x2*)(op + e * 16) = (u32x2){cvtpk(acc[4 * e], acc[4 * e + 1]), cvtpk(acc[4 * e + 2], acc[4 * e + 3])};
  TS(4); WAITV(0); TS(5); TSW(4);
}

extern "C" void kernel_launch(void* const* d_in, const int* in_sizes, int n_in, void* d_out, int out_size, void* d_ws, size_t ws_size, hipStream_t stream) {
  (void)in_sizes; (void)n_in; (void)out_size; (void)ws_size;
  const u16* x = (const u16*)d_in[0]; const u16* ckv = (const u16*)d_in[1]; const u16* wq = (const u16*)d_in[2]; const u16* wk = (const u16*)d_in[3];
  const u16* wv = (const u16*)d_in[4]; const u16* wo = (const u16*)d_in[5]; const u16* w_in = (const u16*)d_in[7]; const u16* wsp = (const u16*)d_in[8];
  const u16* bsp = (const u16*)d_in[9]; const u16* wout = (const u16*)d_in[10];
  char* ws = (char*)d_ws;
  (void)hipFuncSetAttribute((const void*)k0_ln_z, hipFuncAttributeMaxDynamicSharedMemorySize, K0_LDS);
  (void)hipFuncSetAttribute((const void*)k1_qkv, hipFuncAttributeMaxDynamicSharedMemorySize, K1_LDS);
  (void)hipFuncSetAttribute((const void*)k2_attn, hipFuncAttributeMaxDynamicSharedMemorySize, K2_LDS);
  (void)hipFuncSetAttribute((const void*)k3_gemm, hipFuncAttributeMaxDynamicSharedMemorySize, K3_LDS);
  (void)hipFuncSetAttribute((const void*)k4_out, hipFuncAttributeMaxDynamicSharedMemorySize, K4_LDS);
  k0_ln_z<<<240, 512, K0_LDS, stream>>>(x, w_in, ws);
  k1_qkv<<<256, 512, K1_LDS, stream>>>(ckv, wq, wk, wv, ws);
  k2_attn<<<256, 512, K2_LDS, stream>>>(ws);
  k3_gemm<<<224, 512, K3_LDS, stream>>>(wo, wsp, bsp, ws);
  k4_out<<<256, 256, K4_LDS, stream>>>(x, wout, ws, (u16*)d_out);
}
```

```cpp
#include <hip/hip_runtime.h>
#include <hip/hip_bf16.h>
#include <stdint.h>

typedef __bf16 bf16x8 __attribute__((ext_vector_type(8)));
typedef __bf16 bf16x4 __attribute__((ext_vector_type(4)));
typedef __bf16 bf16x2 __attribute__((ext_vector_type(2)));
typedef float f32x16 __attribute__((ext_vector_type(16)));
typedef float f32x4 __attribute__((ext_vector_type(4)));
typedef float f32x2 __attribute__((ext_vector_type(2)));
typedef unsigned u32x4 __attribute__((ext_vector_type(4)));
typedef unsigned u32x2 __attribute__((ext_vector_type(2)));
typedef unsigned short u16;
typedef __amdgpu_buffer_rsrc_t rsrc_t;
#define LAS __attribute__((address_space(3)))
#define DI __device__ __forceinline__
#define SB() __builtin_amdgcn_sched_barrier(0)
#define MFMA(a, b, c) __builtin_amdgcn_mfma_f32_32x32x16_bf16((a), (b), (c), 0, 0, 0)
#define WAITV(n) asm volatile("s_waitcnt vmcnt(" #n ")" ::: "memory")
#define WAITL0() asm volatile("s_waitcnt lgkmcnt(0)" ::: "memory")
#define BAR() do { asm volatile("" ::: "memory"); __builtin_amdgcn_s_barrier(); asm volatile("" ::: "memory"); } while (0)

#define ROT 0
#define AUXK 0
#define AUXB 0
#define PB 2
#define PA 3
#define EXPA 0
#define EXPB 0
#define TSD
#define TS(i)
#define TSW(k)
constexpr int SEQ = 2048, DM = 512, NH = 32, HD = 128, FH = 688, FI = 1376, ZLD = 768;
constexpr size_t WS_XN = 0;
constexpr size_t WS_Z1 = WS_XN + (size_t)SEQ * DM * 2;
constexpr size_t WS_Z2 = WS_Z1 + (size_t)SEQ * FH * 2;
constexpr size_t WS_Z2N = WS_Z2 + (size_t)SEQ * FH * 2;
constexpr size_t WS_H = WS_Z2N + (size_t)SEQ * ZLD * 2;
constexpr size_t WS_Q = 32u << 20;
constexpr size_t WS_K = WS_Q + (16u << 20);
constexpr size_t WS_V = WS_K + (16u << 20);
constexpr size_t WS_C = WS_V + (16u << 20);
constexpr size_t WS_P = WS_C + (16u << 20);
static_assert(WS_H + (size_t)SEQ * FH * 2 <= WS_Q, "ws map");

DI unsigned rfl(unsigned v) { return __builtin_amdgcn_readfirstlane(v); }
DI unsigned cvtpk(float lo, float hi) { f32x2 v = {lo, hi}; bf16x2 b = __builtin_convertvector(v, bf16x2); return __builtin_bit_cast(unsigned, b); }
DI float bflo(unsigned u) { return __uint_as_float(u << 16); }
DI float bfhi(unsigned u) { return __uint_as_float(u & 0xffff0000u); }
DI float rbf(float v) { return bflo(cvtpk(v, v) & 0xffffu); }
template <int S> DI u32x4 pack8(const f32x16& c) {
  u32x4 w = {cvtpk(c[8 * S + 0], c[8 * S + 1]), cvtpk(c[8 * S + 2], c[8 * S + 3]), cvtpk(c[8 * S + 4], c[8 * S + 5]), cvtpk(c[8 * S + 6], c[8 * S + 7])};
  return w;
}
DI rsrc_t mkrsrc(const void* p, unsigned bytes) { return __builtin_amdgcn_make_buffer_rsrc((void*)p, (short)0, (int)bytes, 0x00020000); }
template <int IMM> DI void dma16(rsrc_t r, unsigned voff, unsigned soff, unsigned m0v) {
  asm volatile("s_mov_b32 m0, %0\n\ts_nop 0\n\tbuffer_load_dwordx4 %1, %2, %3 offen offset:%4 lds" ::"s"(m0v), "v"(voff), "s"(r), "s"(soff), "i"(IMM) : "memory");
}
DI bf16x8 ldsr(LAS const char* p) { return *(LAS const bf16x8*)p; }
DI bf16x8 ldstr(LAS const char* p0, LAS const char* p1) {
  bf16x4 a = __builtin_amdgcn_ds_read_tr16_b64_v4bf16((LAS bf16x4*)p0);
  bf16x4 b = __builtin_amdgcn_ds_read_tr16_b64_v4bf16((LAS bf16x4*)p1);
  return __builtin_shufflevector(a, b, 0, 1, 2, 3, 4, 5, 6, 7);
}
DI float swap32_add(float v) { auto r = __builtin_amdgcn_permlane32_swap(__float_as_uint(v), __float_as_uint(v), false, false); return __uint_as_float(r[0]) + __uint_as_float(r[1]); }
DI float swap32_max(float v) { auto r = __builtin_amdgcn_permlane32_swap(__float_as_uint(v), __float_as_uint(v), false, false); return fmaxf(__uint_as_float(r[0]), __uint_as_float(r[1])); }
DI u32x4 natural8(u32x4 w) {
  const auto a = __builtin_amdgcn_permlane32_swap(w[0], w[2], false, false), b = __builtin_amdgcn_permlane32_swap(w[1], w[3], false, false);
  return (u32x4){a[0], b[0], a[1], b[1]};
}
template <int IMM> DI void dma16n(rsrc_t r, unsigned voff, unsigned soff) {
  asm volatile("buffer_load_dwordx4 %0, %1, %2 offen offset:%3 lds" ::"v"(voff), "s"(r), "s"(soff), "i"(IMM) : "memory");
}
template <int AUX = 0> DI void bst16(u32x4 v, rsrc_t r, unsigned vo, unsigned so) { __builtin_amdgcn_raw_buffer_store_b128(v, r, vo, so, AUX); }
#define WAITVL(n) asm volatile("s_waitcnt vmcnt(%0) lgkmcnt(0)" ::"i"(n) : "memory")
DI void ln_stats(float s, float q, float invn, float& mb, float& rs) {
  const float mean = s * invn, var = fmaxf(fmaf(-mean, mean, q * invn), 0.f);
  mb = rbf(mean); rs = 1.f / rbf(sqrtf(rbf(rbf(var) + 1e-5f)));
}
DI float dot2(unsigned a, unsigned b, float c) { return __builtin_amdgcn_fdot2_f32_bf16(__builtin_bit_cast(bf16x2, a), __builtin_bit_cast(bf16x2, b), c, false); }

constexpr int K1_LDS = 163840;
constexpr float QSCALE = 0.08838834764831845f * 1.4426950408889634f;
struct K1C {
  rsrc_t rA, rW0, rW1, rX, rI, rK;
  unsigned voA[2], voB, voI[2], vo, m0A, m0B, soA, soB[2], soX, soK[2]; int rot, rotz;
  LAS const char* aR[4]; LAS const char* bR;
  rsrc_t rP[3], rN; unsigned voP, soP[2], voN, soN; u32x4 xn;
};
#define PFK 37
#define HVX 0
#define NPRO 10
#define QUO 4
struct K1Op { int kind, item, piece; };
constexpr int k1_xslot(int i) { return i % 5 == 0 ? 6 : i % 5 == 1 ? 7 : i == 2 ? 4 : i == 3 ? 5 : i == 4 ? 9 : 4; }
constexpr int k1_wslot(int i) { return i % 5 == 0 ? 2 : i % 5 == 1 ? 3 : i == 3 ? 0 : i == 4 ? 1 : 8; }
constexpr K1Op k1_free(int p, int i) {
  if (p < 0) { const int q = p + 10; constexpr int it[10] = {0, 0, 1, 1, 2, 3, 2, 4, 5, 3}; return i < 2 ? K1Op{q % 3 == 0 ? 2 : 1, it[q], i} : K1Op{0, 0, 0}; }
  if (i < 2) return p < 26 ? K1Op{1, p + 6, i} : p == 26 ? K1Op{4, 0, i} : p == 27 ? K1Op{4, 1, i} : p == 28 ? K1Op{3, 2, i} : p == 29 ? K1Op{3, 3, i} : p == 30 ? K1Op{4, 3, i}
                  : p == 31 ? K1Op{4, 4, i} : p < 35 ? K1Op{3, p - 27, i} : K1Op{0, 0, 0};
  if (i == 2) return p == 0 ? K1Op{0, 0, 0} : p < 25 ? K1Op{2, (p - 1) / 2 + 4, (p - 1) & 1} : p < 29 ? K1Op{3, (p - 25) / 2, (p - 1) & 1} : p < 31 ? K1Op{4, 2, (p - 1) & 1}
                   : p == 31 ? K1Op{3, 4, 0} : p < 35 ? K1Op{4, p - 27, 0} : K1Op{0, 0, 0};
  return p == 31 ? K1Op{3, 4, 1} : p >= 32 && p < 35 ? K1Op{4, p - 27, 1} : K1Op{0, 0, 0};
}
constexpr int k1_cum(int p) {
  int n = 0;
  for (int q = -10; q <= p; ++q) for (int i = 0; i < 4; ++i) n += k1_free(q, i).kind != 0;
  return p < -1 ? 0 : n < NPRO + QUO * (p + 1) ? n : NPRO + QUO * (p + 1);
}
constexpr K1Op k1_seq(int n) {
  for (int q = -10; q < 40; ++q) for (int i = 0; i < 4; ++i) if (k1_free(q, i).kind && n-- == 0) return k1_free(q, i);
  return K1Op{0, 0, 0};
}
constexpr K1Op k1_op(int p, int i) {
  if (p < 0 || i < 4) return k1_cum(p - 1) + i < k1_cum(p) ? k1_seq(k1_cum(p - 1) + i) : p == PFK && i < 3 ? K1Op{6, i, 0} : K1Op{0, 0, 0};
  if (i == 4) return p == 0 ? K1Op{7, 0, 0} : p >= 16 && p < 31 && !(p & 1) ? K1Op{5, 0, (p - 16) / 2} : p == 32 || p == 33 ? K1Op{5, 1, p - 32} : p >= 34 && p < 37 ? K1Op{5, 1, 2 * p - 66} : K1Op{0, 0, 0};
  return p >= 34 && p < 37 ? K1Op{5, 1, 2 * p - 65} : K1Op{0, 0, 0};
}
constexpr bool k1_needs(int p, K1Op o) {
  return p < 32 ? (o.kind == 1 && o.item == p) || (o.kind == 2 && o.item == p / 2) : (o.kind == 3 || o.kind == 4) && o.item == p - 32;
}
constexpr int k1_wait(int p, int upto) {
  int n = 0, got = 0;
  for (int q = -1; q < upto; ++q) for (int i = 0; i < (q < 0 ? 20 : 6); ++i) { const K1Op o = k1_op(q, i); if (k1_needs(p, o)) { n = 0; ++got; } else if (o.kind) ++n; }
  return got == 4 ? n : -1;
}
struct K1A { f32x16 k[2], v[2]; };
template <int P, int I> DI void k1_vm(const K1C& c, const K1A& a0, const K1A& a1, unsigned& pf) {
  constexpr K1Op o = k1_op(P, I);
  if constexpr (o.kind == 1) dma16<0>(o.piece ? c.rW1 : c.rW0, c.voB, c.soB[o.piece] + ((o.item + 2 * c.rot) & 15) * 262144, c.m0B + (o.item % 6) * 16384 + o.piece * 8192);
  if constexpr (o.kind == 2) dma16<0>(c.rA, c.voA[o.piece], c.soA + (o.item >> 3) * 131072 + ((o.item + c.rot) & 7) * 128, c.m0A + (6 + (o.item & 3)) * 16384 + o.piece * 1024);
  if constexpr (o.kind == 3) dma16<0>(c.rX, c.voA[o.piece], c.soX + ((o.item + c.rotz) & 7) * 128, c.m0A + k1_xslot(o.item) * 16384 + o.piece * 1024);
  if constexpr (o.kind == 4) dma16<0>(c.rI, c.voI[o.piece], ((o.item + c.rotz) & 7) * (64 * FI * 2), c.m0A + k1_wslot(o.item) * 16384 + o.piece * 1024);
  if constexpr (o.kind == 5 && !(EXPA == 1 && o.item == 0) && !(EXPA == 2)) {
    const K1A& a = o.item ? a1 : a0;
    const f32x16& t = o.piece & 2 ? a.v[o.piece >> 2] : a.k[o.piece >> 2];
    bst16<AUXK>(pack8<o.piece & 1>(t), c.rK, c.vo, c.soK[(o.piece >> 1) & 1] + o.item * 32768 + (o.piece >> 2) * 8192 + (o.piece & 1) * 1024);
  }
  if constexpr (o.kind == 7) bst16<AUXK>(c.xn, c.rN, c.voN, c.soN);
  if constexpr (o.kind == 6) asm volatile("buffer_load_dword %0, %1, %2, %3 offen" : "+v"(pf) : "v"(c.voP), "s"(c.rP[o.item]), "s"(o.item == 2 ? 0u : c.soP[o.item]) : "memory");
}
struct K1F { bf16x8 a[2][2], w[2][2]; };
template <int P, int G> DI void k1_rd(const K1C& c, K1F& f) {
  if constexpr (P < 32) {
    constexpr int KS = G / 3;
    if constexpr (G % 3 == 1) { LAS const char* a = c.aR[2 * (P & 1) + KS] + ((P >> 1) & 3) * 16384; f.a[KS][0] = ldsr(a); f.a[KS][1] = ldsr(a + 4096); }
    else { LAS const char* b = c.bR + (P % 6) * 16384 + KS * 4096 + (G % 3 / 2) * 8192; f.w[KS][G % 3 / 2] = ldstr(b, b + 1024); }
  }
}
struct K1Z { bf16x8 x[2], w[2][3], d[2]; };
struct K1ZA { LAS const char* xR[2]; LAS const char* wR; LAS const char* dR; f32x16 acc[3]; float s, q, cs; };
template <int S, int K, int G> DI void k1z_rd(const K1ZA& z, K1Z& f) {
  if constexpr (S < 8) {
    constexpr int o = k1_wslot(S) * 16384 + K * 16 * 192;
    f.w[K][G] = ldstr(z.wR + o + G * 64, z.wR + o + G * 64 + 768);
    if constexpr (G == 0) f.x[K] = ldsr(z.xR[K] + k1_xslot(S) * 16384);
    if constexpr (G == 1) f.d[K] = ldstr(z.dR + o, z.dR + o + 768);
  }
}
template <int P> DI void k1_per(const K1C& c, const K1F& f, K1F& fn, K1A& a0, K1A& a1, unsigned& pf, const K1ZA& z, K1Z& g) {
  K1A& a = P < 16 ? a0 : a1;
  static_assert(k1_wait(P + 1, P) >= 0, "operand not requested");
  WAITVL(k1_wait(P + 1, P)); BAR(); SB();
#define KM(ks, i) a.k[i] = MFMA(f.w[ks][0], f.a[ks][i], a.k[i]); SB()
#define VM(ks, i) a.v[i] = MFMA(f.a[ks][i], f.w[ks][1], a.v[i]); SB()
#define KR(G) if constexpr (P < 31) k1_rd<P + 1, G>(c, fn); else k1z_rd<0, (G) / 3, (G) % 3>(z, g)
  KM(0, 0); KR(0); SB(); KM(0, 1); KR(1); SB(); VM(0, 0); KR(2); SB();
  VM(0, 1); KR(3); k1_vm<P, 0>(c, a0, a1, pf); SB(); KM(1, 0); KR(4); k1_vm<P, 1>(c, a0, a1, pf); SB(); KM(1, 1); KR(5); k1_vm<P, 2>(c, a0, a1, pf); SB();
  VM(1, 0); k1_vm<P, 3>(c, a0, a1, pf); SB(); VM(1, 1); k1_vm<P, 4>(c, a0, a1, pf); SB();
}
template <int P, int E> DI void k1_run(const K1C& c, K1F& f0, K1F& f1, K1A& a0, K1A& a1, unsigned& pf, const K1ZA& z, K1Z& g) {
  if constexpr (P < E) { k1_per<P>(c, f0, f1, a0, a1, pf, z, g); k1_per<P + 1>(c, f1, f0, a0, a1, pf, z, g); k1_run<P + 2, E>(c, f0, f1, a0, a1, pf, z, g); }
}
template <int S, int K> DI void k1z_half(const K1C& c, const K1A& a1, K1ZA& z, const K1Z& f, K1Z& fn, unsigned& pf) {
  const u32x4 x = __builtin_bit_cast(u32x4, f.x[K]), d = __builtin_bit_cast(u32x4, f.d[K]);
  z.acc[0] = MFMA(f.w[K][0], f.x[K], z.acc[0]); SB(); k1z_rd<S + 1, K, 0>(z, fn);
#pragma unroll
  for (int e = 0; e < 4; ++e) z.s = dot2(x[e], 0x3f803f80u, z.s);
  k1_vm<32 + S, 3 * K>(c, a1, a1, pf); SB();
  z.acc[1] = MFMA(f.w[K][1], f.x[K], z.acc[1]); SB(); k1z_rd<S + 1, K, 1>(z, fn);
#pragma unroll
  for (int e = 0; e < 4; ++e) z.q = dot2(x[e], x[e], z.q);
  k1_vm<32 + S, 3 * K + 1>(c, a1, a1, pf); SB();
  z.acc[2] = MFMA(f.w[K][2], f.x[K], z.acc[2]); SB(); k1z_rd<S + 1, K, 2>(z, fn);
#pragma unroll
  for (int e = 0; e < 4; ++e) z.cs = dot2(d[e], 0x3f803f80u, z.cs);
  k1_vm<32 + S, 3 * K + 2>(c, a1, a1, pf); SB();
}
template <int S> DI void k1z_stage(const K1C& c, const K1A& a1, K1ZA& z, const K1Z& f, K1Z& fn, unsigned& pf) {
  if constexpr (S < 7) { WAITVL(k1_wait(33 + S, 32 + S)); BAR(); SB(); }
  k1z_half<S, 0>(c, a1, z, f, fn, pf); k1z_half<S, 1>(c, a1, z, f, fn, pf);
}
template <int KH> DI void k1z_send(LAS f32x4* ex, const f32x16 (&acc)[3], f32x4 (&own)[3][2]) {
#pragma unroll
  for (int nb = 0; nb < 3; ++nb)
#pragma unroll
    for (int e = 0; e < 2; ++e) {
      constexpr int o = 8 * KH, t = 8 - o;
      ex[(nb * 2 + e) * 64] = (f32x4){acc[nb][t + 4 * e], acc[nb][t + 4 * e + 1], acc[nb][t + 4 * e + 2], acc[nb][t + 4 * e + 3]};
      own[nb][e] = (f32x4){acc[nb][o + 4 * e], acc[nb][o + 4 * e + 1], acc[nb][o + 4 * e + 2], acc[nb][o + 4 * e + 3]};
      asm volatile("" : "+v"(own[nb][e]));
    }
}
__global__ __launch_bounds__(512, 2) void k1_kvz(const u16* __restrict__ x, const u16* __restrict__ ckv, const u16* __restrict__ wk, const u16* __restrict__ wv,
                                                  const u16* __restrict__ w_in, char* __restrict__ ws, const u16* wq, const u16* wo, const u16* wsp, const u16* wout) {
  extern __shared__ __attribute__((aligned(16))) char lds_[];
  LAS char* lds = (LAS char*)lds_;
  const int tid = threadIdx.x, lane = tid & 63, wid = rfl(tid >> 6), r = lane & 31, h = lane >> 5;
  const int bid = blockIdx.x, xcd = bid & 7, idx = bid >> 3, head = xcd * 4 + (idx >> 3), rb = idx & 7;
  const int wm = wid >> 2, wn = wid & 3, q4 = (lane >> 2) & 3, tt = h ^ ((r >> 1) & 7);
  const int zrb = (xcd >> 1) * 4 + (idx >> 3), cg = (xcd & 1) * 8 + rb;
  TSD; TS(0);
  K1C c;
  K1A a0 = {}, a1 = {};
  unsigned pf = 0;
  const unsigned ldsb = (unsigned)(size_t)lds;
  u32x4 xv;
  c.rX = mkrsrc(x, SEQ * DM * 2);
  asm volatile("buffer_load_dwordx4 %0, %1, %2, %3 offen" : "=v"(xv) : "v"(lane * 16), "s"(c.rX), "s"((bid * 8 + wid) * 1024) : "memory");
  c.rA = mkrsrc(ckv, SEQ * DM * 2); c.rW0 = mkrsrc(wk, DM * 4096 * 2); c.rW1 = mkrsrc(wv, DM * 4096 * 2);
#pragma unroll
  for (int p = 0; p < 2; ++p) c.voA[p] = (8 * p + (lane >> 3)) * 1024 + (((lane & 7) ^ (4 * p + (lane >> 4))) * 16);
  c.voB = (lane >> 4) * 8192 + (((lane & 15) ^ ((lane >> 4) << 2)) * 16);
  c.soA = (rb * 256 + wid * 16) * 1024; c.soB[0] = wid * 4 * 8192 + head * 256; c.soB[1] = wid * 4 * 8192 + (head ^ HVX) * 256;
  c.rot = ROT & 1 ? 0 : (rb + 2 * (idx >> 3)) & 7; c.rotz = ROT & 2 ? 0 : c.rot;
  c.m0A = ldsb + wid * 2048; c.m0B = ldsb + wid * 1024;
#define P2(q) k1_vm<-1, q>(c, a0, a1, pf); k1_vm<-1, q + 1>(c, a0, a1, pf)
  P2(0); P2(2); P2(4); P2(6); P2(8); P2(10); P2(12); P2(14); P2(16); P2(18);
#pragma unroll
  for (int ks = 0; ks < 4; ++ks) c.aR[ks] = lds + 98304 + (wm * 64 + r) * 128 + ((tt ^ (2 * ks)) * 16);
  c.bR = lds + (8 * h + q4) * 256 + ((wn ^ q4) * 64) + ((lane >> 4) & 1) * 32 + (lane & 3) * 8;
  c.rI = mkrsrc(w_in, DM * FI * 2); c.rK = mkrsrc(ws + WS_K, 32u << 20);
  c.soX = (zrb * 128 + wid * 16) * 1024; c.vo = lane * 16;
  c.soK[0] = ((head * 64 + rb * 8 + wm * 2) * 8 + 2 * wn) * 1024; c.soK[1] = (((head ^ HVX) * 64 + rb * 8 + wm * 2) * 8 + 2 * wn) * 1024 + (16u << 20);
#pragma unroll
  for (int i = 0; i < 2; ++i) {
    const int o = (wid * 2 + i) * 64 + lane;
    c.voI[i] = wid < 6 && cg < 15 ? (o / 12) * (FI * 2) + (o % 12) * 16 + cg * 192 : 0x80000000u;
  }
  c.rP[0] = mkrsrc(bid < 128 ? wq : wo, DM * 4096 * 2); c.rP[1] = mkrsrc(wsp, SEQ * SEQ * 2); c.rP[2] = mkrsrc(wout + (bid * 8 + wid < 172 ? bid * 8 + wid : 0) * 2048, 4096);
  c.voP = lane * 64; c.soP[1] = (bid * 8 + wid) * 4096; c.soP[0] = c.soP[1] & 0x3fffff;
  const int wr = wid & 3, kh = wid >> 2;
  K1ZA z; K1Z g0, g1;
#pragma unroll
  for (int k = 0; k < 2; ++k) z.xR[k] = lds + (wr * 32 + r) * 128 + ((tt ^ (2 * (2 * kh + k))) * 16);
  z.wR = lds + (32 * kh + 8 * h + q4) * 192 + ((lane >> 4) & 1) * 32 + (lane & 3) * 8;
  z.dR = z.wR + (wr % 3) * 64;
  TS(1);
  WAITVL(NPRO); SB();
  {
    asm volatile("" : "+v"(xv));
    float s = 0.f, q = 0.f;
#pragma unroll
    for (int e = 0; e < 4; ++e) { s = dot2(xv[e], 0x3f803f80u, s); q = dot2(xv[e], xv[e], q); }
#pragma unroll
    for (int o = 1; o < 32; o <<= 1) { s += __shfl_xor(s, o); q += __shfl_xor(q, o); }
    s = swap32_add(s); q = swap32_add(q);
    float mb, rs; ln_stats(s, q, 1.f / DM, mb, rs);
#pragma unroll
    for (int e = 0; e < 4; ++e) c.xn[e] = cvtpk((bflo(xv[e]) - mb) * rs, (bfhi(xv[e]) - mb) * rs);
    const int row = bid * 8 + wid;
    c.rN = mkrsrc(ws + WS_XN, SEQ * DM * 2); c.voN = (lane >> 1) * 1024 + (lane & 1) * 512; c.soN = (row >> 5) * 32768 + (row & 31) * 16;
  }
  K1F f0, f1;
  SB(); WAITVL(k1_wait(0, 0)); BAR(); SB();
  TS(2);
  k1_rd<0, 0>(c, f0); k1_rd<0, 1>(c, f0); k1_rd<0, 2>(c, f0); k1_rd<0, 3>(c, f0); k1_rd<0, 4>(c, f0); k1_rd<0, 5>(c, f0); SB();
  k1_run<0, 8>(c, f0, f1, a0, a1, pf, z, g0);
  TS(3);
  k1_run<8, 16>(c, f0, f1, a0, a1, pf, z, g0);
  TS(4);
  k1_run<16, 24>(c, f0, f1, a0, a1, pf, z, g0);
  TS(5);
  k1_run<24, 32>(c, f0, f1, a0, a1, pf, z, g0);
  TS(6);
  z.acc[0] = (f32x16){}; z.acc[1] = (f32x16){}; z.acc[2] = (f32x16){}; z.s = 0.f; z.q = 0.f; z.cs = 0.f;
  k1z_stage<0>(c, a1, z, g0, g1, pf); k1z_stage<1>(c, a1, z, g1, g0, pf); k1z_stage<2>(c, a1, z, g0, g1, pf); k1z_stage<3>(c, a1, z, g1, g0, pf);
  k1z_stage<4>(c, a1, z, g0, g1, pf); k1z_stage<5>(c, a1, z, g1, g0, pf); k1z_stage<6>(c, a1, z, g0, g1, pf); k1z_stage<7>(c, a1, z, g1, g0, pf);
  TS(7);
  if (cg < 15) {
    LAS f32x4* ex = (LAS f32x4*)lds;
    LAS f32x2* st = (LAS f32x2*)(lds + 49152);
    LAS float* cs = (LAS float*)(lds + 51200);
    f32x4 own[3][2];
    if (kh) k1z_send<1>(ex + wid * 384 + lane, z.acc, own); else k1z_send<0>(ex + wid * 384 + lane, z.acc, own);
    { const float s = swap32_add(z.s), q = swap32_add(z.q), cc = swap32_add(z.cs); st[wid * 32 + r] = (f32x2){s, q}; cs[wid * 32 + r] = cc; }
    WAITL0(); BAR();
    const int pw = wid ^ 4, rb32 = zrb * 4 + wr;
    const f32x2 s0 = st[wr * 32 + r], s1 = st[(wr + 4) * 32 + r];
    float mb, rs; ln_stats(s0[0] + s1[0], s0[1] + s1[1], 1.f / DM, mb, rs);
    const float mrs = -mb * rs;
    const rsrc_t rz = mkrsrc(ws + WS_Z1, 2 * SEQ * FH * 2);
#pragma unroll
    for (int nb = 0; nb < 3; ++nb) {
      u32x4 w;
#pragma unroll
      for (int e = 0; e < 2; ++e) {
        const f32x4 c0 = *(LAS const f32x4*)(cs + nb * 32 + 16 * kh + 8 * e + 4 * h), c1 = *(LAS const f32x4*)(cs + 128 + nb * 32 + 16 * kh + 8 * e + 4 * h);
        const f32x4 t = (ex[((pw * 3 + nb) * 2 + e) * 64 + lane] + own[nb][e]) * rs + (c0 + c1) * mrs;
        const f32x4 u = t * (t * t * -0.1029432397f - 2.302208198f);
        f32x4 d;
#pragma unroll
        for (int cc = 0; cc < 4; ++cc) d[cc] = __builtin_amdgcn_exp2f(u[cc]);
        d += 1.f;
#pragma unroll
        for (int cc = 0; cc < 4; ++cc) d[cc] = __builtin_amdgcn_rcpf(d[cc]);
        d *= t;
        w[2 * e] = cvtpk(d[0], d[1]); w[2 * e + 1] = cvtpk(d[2], d[3]);
      }
      const int k16 = 2 * (3 * cg + nb) + kh;
      if (k16 < 43) bst16(w, rz, lane * 16, (rb32 * 43 + k16) * 1024);
      else if (k16 < 86) bst16(natural8(w), rz, lane * 16, SEQ * FH * 2 + (rb32 * 43 + k16 - 43) * 1024);
    }
  }
  TS(8); WAITV(0);
  asm volatile("" ::"v"(pf));
  TS(9); TSW(1);
}

constexpr int K2_LDS = 163840;
constexpr float THR = 32.f;
struct K2C {
  rsrc_t rK, rV, rC;
  unsigned vo, ldsb;
  unsigned mS, mP;
  int n1, g, qrow;
  int rowA, rowB;
};
DI int k2_tile(const K2C& c, int n) { n = n > 16 ? 16 : n; return n < c.n1 ? n : 2 * n - c.n1 + c.g; }
DI unsigned k2_so(const K2C& c, int n) { return (n < c.n1 ? c.mS : (c.mP & 0xffffu)) + (unsigned)k2_tile(c, n) * 16384u; }
DI unsigned k2_m0(const K2C& c, int n) { return c.ldsb + (n < c.n1 ? c.mS : c.mP); }
DI LAS const char* k2_rd(const K2C& c, int n) { return (LAS const char*)(size_t)(c.vo + (c.ldsb + (n < c.n1 ? 0u : (c.mP & 0x10000u)))); }
template <int IMM> DI void dma16c(rsrc_t r, unsigned voff, unsigned soff, int a, int b) {
  asm volatile("s_cmp_lt_i32 %4, %5\n\ts_cbranch_scc1 .Ld%=\n\tbuffer_load_dwordx4 %0, %1, %2 offen offset:%3 lds\n.Ld%=:" ::"v"(voff), "s"(r), "s"(soff), "i"(IMM), "s"(a), "s"(b) : "scc", "memory");
}
DI void k2_mask(f32x16 (&s)[2], int dq) {
#pragma unroll
  for (int kb = 0; kb < 2; ++kb)
#pragma unroll
    for (int e = 0; e < 16; ++e) { const int cc = 32 * kb + (e & 3) + 8 * (e >> 2); if (cc > dq) s[kb][e] = -__builtin_inff(); }
}
DI float max3(float a, float b, float c) { float d; asm("v_max3_f32 %0, %1, %2, %3" : "=v"(d) : "v"(a), "v"(b), "v"(c)); return d; }
DI float k2_max(const f32x16 (&s)[2]) {
  float m0 = fmaxf(s[0][0], s[0][1]), m1 = fmaxf(s[1][0], s[1][1]);
#pragma unroll
  for (int e = 2; e < 16; e += 2) { m0 = fmaxf(fmaxf(m0, s[0][e]), s[0][e + 1]); m1 = fmaxf(fmaxf(m1, s[1][e]), s[1][e + 1]); }
  return fmaxf(m0, m1);
}
DI void addf(float& a, float b) { asm("v_add_f32 %0, %0, %1" : "+v"(a) : "v"(b)); }
DI void k2_loadq(u32x4 (&q)[8], unsigned la, int a, int b) {
  asm volatile("s_cmp_lg_u32 %9, %10\n\ts_cbranch_scc1 .Lq%=\n\t"
               "ds_read_b128 %0, %8\n\tds_read_b128 %1, %8 offset:1024\n\tds_read_b128 %2, %8 offset:2048\n\tds_read_b128 %3, %8 offset:3072\n\t"
               "ds_read_b128 %4, %8 offset:4096\n\tds_read_b128 %5, %8 offset:5120\n\tds_read_b128 %6, %8 offset:6144\n\tds_read_b128 %7, %8 offset:7168\n"
               ".Lq%=:" : "+v"(q[0]), "+v"(q[1]), "+v"(q[2]), "+v"(q[3]), "+v"(q[4]), "+v"(q[5]), "+v"(q[6]), "+v"(q[7])
               : "v"(la), "s"(a), "s"(b) : "scc", "memory");
}
struct K2S { f32x16 o[4], negm; u32x4 q[8]; bf16x8 f[3]; LAS const char* rk; float m, l; };

DI void k2_rescale(K2S& S, f32x16 (&sn)[2], float lmax, bool first) {
  float dl = swap32_max(lmax);
  if (first) { dl = dl == -__builtin_inff() ? 0.f : dl; S.m = dl; }
  else {
    dl = fmaxf(dl, 0.f); S.m += dl;
    const float f = __builtin_amdgcn_exp2f(-dl);
    S.l *= f;
#pragma unroll
    for (int db = 0; db < 4; ++db) S.o[db] *= f;
  }
#pragma unroll
  for (int kb = 0; kb < 2; ++kb) sn[kb] -= dl;
#pragma unroll
  for (int e = 0; e < 16; ++e) S.negm[e] = -S.m;
}
DI void k2_finishB(const K2C& c, K2S& S, unsigned ctx) {
  const float inv = __builtin_amdgcn_rcpf(swap32_add(S.l));
#pragma unroll
  for (int db = 0; db < 4; ++db) {
    const f32x16 res = S.o[db] * inv;
    bst16<AUXB>(pack8<0>(res), c.rC, c.vo, ctx + db * 2048); bst16<AUXB>(pack8<1>(res), c.rC, c.vo, ctx + db * 2048 + 1024);
    S.o[db] = (f32x16){};
  }
  S.l = 0.f;
}
DI void k2_finishA(const K2C& c, K2S& S, LAS char* lds, int wid, unsigned ctx) {
  const int lane = c.vo >> 4;
  const int g = wid >> 2, pw = wid ^ 4;
  LAS f32x4* ex = (LAS f32x4*)lds;
  LAS f32x2* es = (LAS f32x2*)(lds + 65536);
  const float lt = swap32_add(S.l);
  const float me = lt > 0.f ? S.m : -1e30f;
  es[wid * 64 + lane] = (f32x2){me, lt};
#pragma unroll
  for (int rd = 0; rd < 2; ++rd) {
    const f32x16 snd = g ? S.o[rd] : S.o[2 + rd];
#pragma unroll
    for (int e = 0; e < 4; ++e) ex[(wid * 8 + rd * 4 + e) * 64 + lane] = (f32x4){snd[4 * e], snd[4 * e + 1], snd[4 * e + 2], snd[4 * e + 3]};
  }
  WAITL0(); BAR();
  const f32x2 ps = es[pw * 64 + lane];
  const float mm = fmaxf(me, ps[0]);
  float w0 = __builtin_amdgcn_exp2f(me - mm), w1 = __builtin_amdgcn_exp2f(ps[0] - mm);
  const float inv = __builtin_amdgcn_rcpf(w0 * lt + w1 * ps[1]);
  w0 *= inv; w1 *= inv;
#pragma unroll
  for (int rd = 0; rd < 2; ++rd) {
    const f32x16 own = g ? S.o[2 + rd] : S.o[rd];
    f32x16 res;
#pragma unroll
    for (int e = 0; e < 4; ++e) {
      const f32x4 v = ex[(pw * 8 + rd * 4 + e) * 64 + lane];
#pragma unroll
      for (int c = 0; c < 4; ++c) res[4 * e + c] = own[4 * e + c] * w0 + v[c] * w1;
    }
    const unsigned p = ctx + (2 * g + rd) * 2048;
    bst16(pack8<0>(res), c.rC, c.vo, p); bst16(pack8<1>(res), c.rC, c.vo, p + 1024);
  }
}
template <int PAR> DI bf16x8 k2_frag(LAS const char* rd, int i) {
  if (i < 16) return ldsr(rd + (PAR ^ 1) * 16384 + i * 1024);
  if (i >= 32) return ldsr(rd + PAR * 16384 + (i - 32) * 1024);
  const int ks = (i - 16) >> 2, db = i & 3;
  return ldsr(rd + 32768 + PAR * 16384 + (ks >> 1) * 8192 + db * 2048 + (ks & 1) * 1024);
}
template <int PAR, bool NEXT> DI void k2_step(const K2C& c, K2S& S, int n, f32x16 (&sc)[2], f32x16 (&sn)[2], unsigned qnext, unsigned ctxB) {
  bf16x8 F[35];
  F[0] = S.f[0]; F[1] = S.f[1]; F[2] = S.f[2];
  u32x4 p[4];
  float l0 = S.l, l1 = 0.f, lmax = 0.f;
  unsigned soK = 0, soV = 0, soK2 = 0;
  LAS const char* rv = nullptr;
  if constexpr (NEXT) { soV = k2_so(c, n + 1); soK = k2_so(c, n + 3); soK2 = k2_so(c, n + 2); }
#pragma unroll
  for (int s = 0; s < 32; ++s) {
    if (s == 29) { asm volatile("s_waitcnt vmcnt(0) lgkmcnt(0)" ::: "memory"); BAR(); if (EXPB & 4) __builtin_amdgcn_s_sleep(4); if (EXPB & 8) __builtin_amdgcn_s_sleep(8); SB(); }
    if (s < 16) { if constexpr (NEXT) sn[s >> 3] = MFMA(F[s], __builtin_bit_cast(bf16x8, S.q[s & 7]), (s & 7) == 0 ? S.negm : sn[s >> 3]); }
    else S.o[s & 3] = MFMA(F[s], __builtin_bit_cast(bf16x8, p[(s - 16) >> 2]), S.o[s & 3]);
    SB();
    if (s == 0 && NEXT) F[3] = k2_frag<PAR>(S.rk, 3);
    if (s <= 27 && (NEXT || s >= 12)) F[s + 4] = (EXPB & 1) && (s & 1) ? F[s + 3] : k2_frag<PAR>(s < 12 ? S.rk : rv, s + 4);
    if (s >= 29 && NEXT) F[s + 3] = k2_frag<PAR>(S.rk, s + 3);
    if (s == 9) rv = k2_rd(c, n);
    if (s == 18 && NEXT) S.rk = k2_rd(c, n + 2);
    const int e0 = s < 5 ? 2 * s : s + 5, e1 = s < 5 ? 2 * s + 2 : s + 6;
    const int a0 = s < 6 ? 2 * s - 2 : s + 4, a1 = s < 6 ? 2 * s : s + 5;
    if (s <= 26) {
#pragma unroll
      for (int e = e0; e < e1; ++e) if (!(EXPB & 2) || (e & 1)) sc[e >> 4][e & 15] = __builtin_amdgcn_exp2f(sc[e >> 4][e & 15]);
    }
    if (s >= 1 && s <= 27) {
#pragma unroll
      for (int e = a0; e < a1; ++e) {
        if (e & 1) { addf(l1, sc[e >> 4][e & 15]); p[e >> 3][(e >> 1) & 3] = cvtpk(sc[e >> 4][(e & 15) - 1], sc[e >> 4][e & 15]); }
        else addf(l0, sc[e >> 4][e & 15]);
      }
    }
    if constexpr (NEXT) {
      if (s == 10) lmax = max3(sn[0][0], sn[0][1], sn[0][1]);
      if (s > 10 && s <= 17) lmax = max3(lmax, sn[0][2 * (s - 10)], sn[0][2 * (s - 10) + 1]);
      if (s >= 19 && s <= 26) lmax = max3(lmax, sn[1][2 * (s - 19)], sn[1][2 * (s - 19) + 1]);
      if (s == 1) dma16c<2048>(c.rK, c.vo, soK2, n + 2, c.n1);
      if (s == 3) dma16c<3072>(c.rK, c.vo, soK2, n + 2, c.n1);
      if (s == 5) dma16<0>(c.rV, c.vo, soV, k2_m0(c, n + 1) + 32768 + (PAR ^ 1) * 16384);
      if (s == 7) dma16n<1024>(c.rV, c.vo, soV);
      if (s == 9) dma16c<2048>(c.rV, c.vo, soV, n + 1, c.n1);
      if (s == 11) dma16c<3072>(c.rV, c.vo, soV, n + 1, c.n1);
      if (s == 16) k2_loadq(S.q, qnext, n + 2, c.g ? c.n1 : -1);
      if (s == 29) { dma16<0>(c.rK, c.vo, soK, k2_m0(c, n + 3) + (PAR ^ 1) * 16384); }
      if (s == 31) { dma16n<1024>(c.rK, c.vo, soK); }
    }
    if (s == 28) { S.l = l0 + l1; asm volatile("" : "+v"(S.l), "+v"(lmax)); }
    SB();
  }
  if constexpr (NEXT) {
    S.f[0] = F[32]; S.f[1] = F[33]; S.f[2] = F[34];
    const bool first = c.g && n + 1 == c.n1;
    const int kb = k2_tile(c, n + 1) * 64, rw = n + 1 < c.n1 ? c.rowB : c.rowA;
    if (kb + 63 > rw) { k2_mask(sn, rw + c.qrow - kb); lmax = k2_max(sn); }
    if (first || __any(lmax > THR)) k2_rescale(S, sn, lmax, first);
    if (first) k2_finishB(c, S, ctxB);
    if (c.g && n + 2 == c.n1) {
#pragma unroll
      for (int e = 0; e < 16; ++e) S.negm[e] = 0.f;
    }
    SB();
  }
}
struct K2Q { rsrc_t rX, rW; unsigned voB, soX, soW, m0A, m0B; int rot; LAS const char* aR; LAS const char* bR[2]; };
struct K2F { bf16x8 a[2][2], w[2][2]; };
template <int ST, int G> DI void k2q_rd(const K2Q& q, K2F& f) {
  constexpr int KS = G / 3, o = ST * 24576;
  if constexpr (G % 3 == 1) { f.a[KS][0] = ldsr(q.aR + o + KS * 1024); f.a[KS][1] = ldsr(q.aR + o + 2048 + KS * 1024); }
  else { constexpr int i = G % 3 / 2; f.w[KS][i] = ldstr(q.bR[i] + o + KS * 4096, q.bR[i] + o + KS * 4096 + 1024); }
}
template <int N> DI void ln_part(const u32x4 (&v)[N], LAS f32x2* st, int wid, int lane) {
  float s = 0.f, q = 0.f;
#pragma unroll
  for (int i = 0; i < N; ++i)
#pragma unroll
    for (int e = 0; e < 4; ++e) { const float a = bflo(v[i][e]), b = bfhi(v[i][e]); s += a + b; q = fmaf(a, a, fmaf(b, b, q)); }
  s += __shfl_xor(s, 8); q += __shfl_xor(q, 8); s += __shfl_xor(s, 16); q += __shfl_xor(q, 16);
  s = swap32_add(s); q = swap32_add(q);
  if (lane < 8) st[lane * 8 + wid] = (f32x2){s, q};
}
template <int N> DI void ln_apply(u32x4 (&v)[N], LAS const f32x2* st, int lane, float invn) {
  float s = 0.f, q = 0.f;
#pragma unroll
  for (int i = 0; i < 4; ++i) { const f32x4 t = *(LAS const f32x4*)(st + (lane & 7) * 8 + 2 * i); s += t[0] + t[2]; q += t[1] + t[3]; }
  float mb, rs; ln_stats(s, q, invn, mb, rs);
#pragma unroll
  for (int i = 0; i < N; ++i)
#pragma unroll
    for (int e = 0; e < 4; ++e) v[i][e] = cvtpk((bflo(v[i][e]) - mb) * rs, (bfhi(v[i][e]) - mb) * rs);
}
__global__ __launch_bounds__(512, 2) void k2_attn(const u16* __restrict__ wq_, char* __restrict__ ws) {
  extern __shared__ __attribute__((aligned(16))) char lds_[];
  LAS char* lds = (LAS char*)lds_;
  const int tid = threadIdx.x, lane = tid & 63, wid = rfl(tid >> 6), r = lane & 31, h = lane >> 5;
  const int bid = blockIdx.x, idx = bid >> 3, head = (bid & 7) * 4 + (idx >> 3), j = idx & 7;
  const int g = wid >> 2, wq = wid & 3;
  TSD; TS(0);
  K2C c;
  c.rK = mkrsrc(ws + WS_K + (size_t)head * 524288, 524288); c.rV = mkrsrc(ws + WS_V + (size_t)head * 524288, 524288);
  c.rC = mkrsrc(ws + WS_C, 16u << 20);
  c.vo = lane * 16; c.ldsb = (unsigned)(size_t)lds; c.mS = wid * 2048; c.mP = g * 65536 + wq * 4096;
  c.n1 = 2 * j + 2; c.g = g; c.qrow = r - 4 * h;
  const int rbA = (15 - j) * 4 + wq, rbB = g ? j * 4 + wq : rbA;
  c.rowA = rbA * 32; c.rowB = rbB * 32;
  const unsigned ctxA = (rbA * 32 + head) * 8192, ctxB = (rbB * 32 + head) * 8192;
  K2S S;
  {
    u32x4 zv[2];
    const int ch = tid >> 3, zr = bid * 8 + (lane & 7);
    {
      const rsrc_t rz = mkrsrc(ws + WS_Z2, SEQ * FH * 2);
      const unsigned zo = (zr >> 5) * 43 * 1024 + (ch >> 1) * 1024 + (ch & 1) * 512 + (zr & 31) * 16;
      asm volatile("buffer_load_dwordx4 %0, %1, %2, 0 offen" : "=v"(zv[0]) : "v"(zo), "s"(rz) : "memory");
      asm volatile("buffer_load_dwordx4 %0, %1, %2, 0 offen" : "=v"(zv[1]) : "v"(ch < 22 ? zo + 32768 : 0x80000000u), "s"(rz) : "memory");
    }
    LAS f32x2* st = (LAS f32x2*)(lds + 49152);
    K2Q q;
    q.rX = mkrsrc(ws + WS_XN, SEQ * DM * 2); q.rW = mkrsrc(wq_, DM * 4096 * 2);
    q.voB = (lane >> 4) * 8192 + (((lane & 15) ^ ((lane >> 4) << 2)) * 16);
    q.soX = rbB * 32768; q.soW = wid * 4 * 8192 + head * 256;
    q.m0A = c.ldsb + 65536 + wid * 2048; q.m0B = c.ldsb + 65536 + 16384 + wid * 1024;
    const int wm = wid >> 1, wn = wid & 1, q4 = (lane >> 2) & 3;
    q.aR = lds + 65536 + wm * 4096 + lane * 16;
#pragma unroll
    for (int i = 0; i < 2; ++i) q.bR[i] = lds + 65536 + 16384 + (8 * h + q4) * 256 + (((2 * wn + i) ^ q4) * 64) + ((lane >> 4) & 1) * 32 + (lane & 3) * 8;
#define QX0(ST, t) dma16<0>(q.rX, c.vo, q.soX + (((t) + q.rot) & 15) * 2048, q.m0A + (ST) * 24576)
#define QX1(t) dma16n<1024>(q.rX, c.vo, q.soX + (((t) + q.rot) & 15) * 2048)
#define QW(ST, t) dma16<0>(q.rW, q.voB, q.soW + (((t) + q.rot) & 15) * 262144, q.m0B + (ST) * 24576)
#define QM(f, k, i, cb) S.o[2 * (i) + (cb)] = MFMA(f.w[k][cb], f.a[k][i], S.o[2 * (i) + (cb)])
#define QRD(ST, t, G, fn) if ((t) < 15) k2q_rd<((ST) + 1) & 3, G>(q, fn)
#define K2Q_STAGE(ST, t, VW, f, fn, D0, D1, D2) do { WAITVL(VW); BAR(); SB(); \
    QM(f, 0, 0, 0); SB(); QRD(ST, t, 0, fn); SB(); QM(f, 0, 0, 1); SB(); QRD(ST, t, 1, fn); SB(); QM(f, 0, 1, 0); SB(); QRD(ST, t, 2, fn); SB(); \
    QM(f, 0, 1, 1); SB(); QRD(ST, t, 3, fn); D0; SB(); QM(f, 1, 0, 0); SB(); QRD(ST, t, 4, fn); SB(); QM(f, 1, 0, 1); SB(); QRD(ST, t, 5, fn); D1; SB(); \
    QM(f, 1, 1, 0); SB(); D2; SB(); QM(f, 1, 1, 1); SB(); } while (0)
    q.rot = ROT & 4 ? (2 * j + 4 * (idx >> 3)) & 15 : 0;
    QX0(0, 0); QX1(0); QW(0, 0); QX0(1, 1); QX1(1); QW(1, 1); QX0(2, 2); QX1(2); QW(2, 2); QX0(3, 3); QX1(3); QW(3, 3);
    K2F f0, f1;
#pragma unroll
    for (int i = 0; i < 4; ++i) S.o[i] = (f32x16){};
    TS(1);
    WAITV(9); BAR(); SB();
    TS(2);
    asm volatile("" : "+v"(zv[0]), "+v"(zv[1]));
    k2q_rd<0, 0>(q, f0); k2q_rd<0, 1>(q, f0); k2q_rd<0, 2>(q, f0); k2q_rd<0, 3>(q, f0); k2q_rd<0, 4>(q, f0); k2q_rd<0, 5>(q, f0); SB();
    ln_part<2>(zv, st, wid, lane); SB();
    K2Q_STAGE(0, 0, 6, f0, f1, {
      ln_apply<2>(zv, st, lane, 1.f / FH);
      const rsrc_t ro = mkrsrc(ws + WS_Z2N, SEQ * ZLD * 2);
      const unsigned oo = ((zr & ~12) | ((zr & 4) << 1) | ((zr & 8) >> 1)) * (ZLD * 2) + ch * 16;
      if (ch >= 22) zv[1] = ((u32x4){0u, 0u, 0u, 0u});
      bst16(zv[0], ro, oo, 0); bst16(zv[1], ro, ch < 32 ? oo + 1024 : 0x80000000u, 0);
      SB(); QX0(0, 4); }, QX1(4), QW(0, 4));
    K2Q_STAGE(1, 1, 8, f1, f0, QX0(1, 5), QX1(5), QW(1, 5));
    K2Q_STAGE(2, 2, 8, f0, f1, QX0(2, 6), QX1(6), QW(2, 6));
    K2Q_STAGE(3, 3, 6, f1, f0, QX0(3, 7), QX1(7), QW(3, 7));
    for (int t = 4; t < 12; t += 4) {
      K2Q_STAGE(0, t, 6, f0, f1, QX0(0, t + 4), QX1(t + 4), QW(0, t + 4));
      K2Q_STAGE(1, t + 1, 6, f1, f0, QX0(1, t + 5), QX1(t + 5), QW(1, t + 5));
      K2Q_STAGE(2, t + 2, 6, f0, f1, QX0(2, t + 6), QX1(t + 6), QW(2, t + 6));
      K2Q_STAGE(3, t + 3, 6, f1, f0, QX0(3, t + 7), QX1(t + 7), QW(3, t + 7));
    }
    K2Q_STAGE(0, 12, 6, f0, f1, dma16<0>(c.rK, c.vo, c.mS, c.ldsb + c.mS), dma16n<1024>(c.rK, c.vo, c.mS), {});
    K2Q_STAGE(1, 13, 5, f1, f0, dma16<0>(c.rK, c.vo, c.mS + 16384, c.ldsb + c.mS + 16384), dma16n<1024>(c.rK, c.vo, c.mS + 16384), {});
    K2Q_STAGE(2, 14, 4, f0, f1, dma16<0>(c.rV, c.vo, c.mS, c.ldsb + c.mS + 32768), dma16n<1024>(c.rV, c.vo, c.mS), {});
    K2Q_STAGE(3, 15, 6, f1, f0, {}, {}, {});
    LAS char* qw = lds + (wm < 2 ? 131072 + wm * 16384 : 65536 + (wm - 2) * 16384) + wn * 4096 + lane * 16;
#pragma unroll
    for (int i = 0; i < 4; ++i) {
      S.o[i] *= QSCALE;
      *(LAS u32x4*)(qw + (i >> 1) * 8192 + (i & 1) * 2048) = pack8<0>(S.o[i]); *(LAS u32x4*)(qw + (i >> 1) * 8192 + (i & 1) * 2048 + 1024) = pack8<1>(S.o[i]);
      S.o[i] = (f32x16){};
    }
  }
  S.negm = (f32x16){}; S.m = 0.f; S.l = 0.f;
  f32x16 sa[2], sb[2];
  TS(3);
  asm volatile("s_waitcnt vmcnt(4) lgkmcnt(0)" ::: "memory"); BAR(); SB();
  const unsigned qA = c.ldsb + 131072 + wq * 8192 + c.vo;
#pragma unroll
  for (int i = 0; i < 8; ++i) S.q[i] = *(LAS const u32x4*)(lds + (g ? 65536 : 131072) + wq * 8192 + i * 1024 + lane * 16);
  S.rk = lds + lane * 16;
#pragma unroll
  for (int s = 0; s < 16; ++s) sa[s >> 3] = MFMA(k2_frag<1>(S.rk, s), __builtin_bit_cast(bf16x8, S.q[s & 7]), (s & 7) == 0 ? S.negm : sa[s >> 3]);
  if (63 > c.rowB) k2_mask(sa, c.rowB + c.qrow);
  k2_rescale(S, sa, k2_max(sa), true);
  SB();
  asm volatile("s_waitcnt vmcnt(0) lgkmcnt(0)" ::: "memory"); BAR(); SB();
  dma16<0>(c.rK, c.vo, k2_so(c, 2), k2_m0(c, 2)); dma16n<1024>(c.rK, c.vo, k2_so(c, 2));
#pragma unroll
  for (int i = 0; i < 3; ++i) S.f[i] = k2_frag<0>(S.rk, i);
  TS(4);
  for (int n = 0; n < 16; n += 2) {
    k2_step<0, true>(c, S, n, sa, sb, qA, ctxB);
    k2_step<1, true>(c, S, n + 1, sb, sa, qA, ctxB);
  }
  TS(5);
  k2_step<0, false>(c, S, 16, sa, sb, qA, ctxB);
  TS(6);
  k2_finishA(c, S, lds, wid, ctxA);
  TS(7); WAITV(0); TS(8); TSW(2);
}

constexpr int K3_LDS = 163840;
constexpr int k3_cum(int t, int pro, int ring) { const int a = pro + 2 * (t + 1), b = t + ring + 1; return a < b ? a : b < 32 ? b : 32; }
#define K3ROT(T) (((T) & ~7) | (((T) + c.rot) & 7))
struct GC { rsrc_t rA, rB; unsigned vo, voB[2], soA, soB, m0; LAS const char* bR[4]; };
struct GR { u32x4 a[3][4]; bf16x8 w[2][4]; };
template <int NP, int LDB, int T, int P> DI void gc_dma(const GC& c) {
  if constexpr (T < NP) { if constexpr (P == 0) dma16<0>(c.rB, c.voB[0], c.soB + T * 64 * LDB, c.m0 + (T & 3) * 16384); else dma16n<1024>(c.rB, c.voB[1], c.soB + T * 64 * LDB); }
}
template <int NP, int T, int KS> DI void gc_lda(const GC& c, GR& g) {
  if constexpr (T < NP) asm volatile("buffer_load_dwordx4 %0, %1, %2, %3 offen offset:%4" : "=v"(g.a[T % 3][KS]) : "v"(c.vo), "s"(c.rA), "s"(c.soA + T * 4096), "i"(KS * 1024) : "memory");
}
template <int NP, int HO, int N, int J> DI void gc_rd(const GC& c, GR& g) {
  if constexpr (N < 4 * NP) { LAS const char* b = c.bR[J] + ((N >> 2) & 3) * 16384 + (N & 3) * 4096; g.w[N & 1][J] = ldstr(b, b + HO); }
}
template <int NP, int LDB, int HO, int N, class X> DI void gc_step(const GC& c, GR& g, f32x16 (&o)[4], X& x) {
  constexpr int T = N >> 2, KS = N & 3;
  if constexpr (KS == 3 && T + 1 < NP) {
    WAITVL((T == 0 ? 4 : T + 3 < NP ? 2 : 0) + (T + 2 < NP ? 4 : 0) + X::extra(T)); BAR();
    asm volatile("" : "+v"(g.a[(T + 1) % 3][0]), "+v"(g.a[(T + 1) % 3][1]), "+v"(g.a[(T + 1) % 3][2]), "+v"(g.a[(T + 1) % 3][3])); SB();
  }
  const bf16x8 a = __builtin_bit_cast(bf16x8, g.a[T % 3][KS]);
  o[0] = MFMA(g.w[N & 1][0], a, o[0]); SB(); gc_rd<NP, HO, N + 1, 0>(c, g); SB();
  o[1] = MFMA(g.w[N & 1][1], a, o[1]); SB(); gc_rd<NP, HO, N + 1, 1>(c, g);
  if constexpr (KS == 3) gc_dma<NP, LDB, T + 4, 0>(c); else gc_lda<NP, T + 2, KS>(c, g);
  SB();
  o[2] = MFMA(g.w[N & 1][2], a, o[2]); SB(); gc_rd<NP, HO, N + 1, 2>(c, g); x.template at<N>(); SB();
  o[3] = MFMA(g.w[N & 1][3], a, o[3]); SB(); gc_rd<NP, HO, N + 1, 3>(c, g);
  if constexpr (KS == 3) gc_dma<NP, LDB, T + 4, 1>(c); else if constexpr (KS == 2) gc_lda<NP, T + 2, 3>(c, g);
  SB();
}
template <int NP, int LDB, int HO, int N, class X> DI void gc_run(const GC& c, GR& g, f32x16 (&o)[4], X& x) {
  if constexpr (N < 4 * NP) { gc_step<NP, LDB, HO, N>(c, g, o, x); gc_run<NP, LDB, HO, N + 1>(c, g, o, x); }
}
template <int NP, int LDB> DI void gc_start(const GC& c, GR& g) {
  gc_dma<NP, LDB, 0, 0>(c); gc_dma<NP, LDB, 0, 1>(c); gc_lda<NP, 0, 0>(c, g); gc_lda<NP, 0, 1>(c, g); gc_lda<NP, 0, 2>(c, g); gc_lda<NP, 0, 3>(c, g);
  gc_dma<NP, LDB, 1, 0>(c); gc_dma<NP, LDB, 1, 1>(c); gc_lda<NP, 1, 0>(c, g); gc_lda<NP, 1, 1>(c, g); gc_lda<NP, 1, 2>(c, g); gc_lda<NP, 1, 3>(c, g);
  gc_dma<NP, LDB, 2, 0>(c); gc_dma<NP, LDB, 2, 1>(c); gc_dma<NP, LDB, 3, 0>(c); gc_dma<NP, LDB, 3, 1>(c);
}
template <int NP, int HO> DI void gc_first(const GC& c, GR& g) {
  asm volatile("" : "+v"(g.a[0][0]), "+v"(g.a[0][1]), "+v"(g.a[0][2]), "+v"(g.a[0][3]));
  gc_rd<NP, HO, 0, 0>(c, g); gc_rd<NP, HO, 0, 1>(c, g); gc_rd<NP, HO, 0, 2>(c, g); gc_rd<NP, HO, 0, 3>(c, g); SB();
}
struct GNone { static constexpr int extra(int) { return 0; } template <int N> DI void at() {} };
struct K3B { rsrc_t rA, rB; unsigned voA[2], voB[2], soA, m0; int rot; LAS const char* aR; LAS const char* bR; };
struct K3F { bf16x8 a[2], w[3]; };
template <int T, int P> DI void k3b_dma(const K3B& c) {
  if constexpr (T < 32) {
    if constexpr (P == 0) dma16<0>(c.rA, c.voA[0], c.soA + K3ROT(T) * 128, c.m0 + (T % 5) * 32768);
    if constexpr (P == 1) dma16n<1024>(c.rA, c.voA[1], c.soA + K3ROT(T) * 128);
    if constexpr (P == 2) dma16<0>(c.rB, c.voB[0], K3ROT(T) * (64 * ZLD * 2), c.m0 + (T % 5) * 32768 + 16384);
    if constexpr (P == 3) dma16n<1024>(c.rB, c.voB[1], K3ROT(T) * (64 * ZLD * 2));
  }
}
template <int T, int G> DI void k3b_rd(const K3B& c, K3F& f) {
  if constexpr (T < 32) {
    constexpr int o = (T % 5) * 32768;
    if constexpr (G == 1) { f.a[0] = ldsr(c.aR + o); f.a[1] = ldsr(c.aR + o + 4096); }
    else { constexpr int j = G ? G - 1 : 0; f.w[j] = ldstr(c.bR + o + j * 64, c.bR + o + j * 64 + 8 * 192); }
  }
}
template <int T> DI void k3b_stage(const K3B& c, const K3F& f, K3F& fn, f32x16 (&o)[6]) {
  constexpr int N = k3_cum(T - 1, PB, 5), M = k3_cum(T, PB, 5) - N == 2 ? N + 1 : 32;
  if constexpr (T < 31) { WAITVL(4 * (N - T - 2) + (T + 2 <= PB ? 4 : 0)); BAR(); SB(); }
#define BM(i, j) o[3 * (i) + (j)] = MFMA(f.w[j], f.a[i], o[3 * (i) + (j)]); SB()
  BM(0, 0); k3b_rd<T + 1, 0>(c, fn); SB(); BM(1, 0); k3b_rd<T + 1, 1>(c, fn); k3b_dma<N, 0>(c); SB(); BM(0, 1); k3b_rd<T + 1, 2>(c, fn); k3b_dma<N, 1>(c); SB();
  BM(1, 1); k3b_rd<T + 1, 3>(c, fn); k3b_dma<N, 2>(c); SB(); BM(0, 2); k3b_dma<N, 3>(c); k3b_dma<M, 0>(c); k3b_dma<M, 1>(c); SB(); BM(1, 2); k3b_dma<M, 2>(c); k3b_dma<M, 3>(c); SB();
}
template <int T> DI void k3b_run(const K3B& c, K3F& f0, K3F& f1, f32x16 (&o)[6]) {
  if constexpr (T < 32) { k3b_stage<T>(c, f0, f1, o); k3b_stage<T + 1>(c, f1, f0, o); k3b_run<T + 2>(c, f0, f1, o); }
}
template <int S> DI void unit_put(LAS char* p, const f32x16& a) {
  *(LAS f32x4*)p = (f32x4){a[8 * S], a[8 * S + 1], a[8 * S + 2], a[8 * S + 3]}; *(LAS f32x4*)(p + 1024) = (f32x4){a[8 * S + 4], a[8 * S + 5], a[8 * S + 6], a[8 * S + 7]};
}
template <int KG, int J> DI void unit_send(LAS char* ex, const f32x16 (&o)[2 * J], float (&v)[J][8]) {
#pragma unroll
  for (int u = 0; u < 4; ++u)
#pragma unroll
    for (int j = 0; j < J; ++j) {
      if (u == KG) {
#pragma unroll
        for (int e = 0; e < 8; ++e) { v[j][e] = o[J * (u >> 1) + j][8 * (u & 1) + e]; asm volatile("" : "+v"(v[j][e])); }
      } else {
        LAS char* p = ex + ((u * 3 + (KG > u ? KG - 1 : KG)) * J + j) * 2048;
        if (u & 1) unit_put<1>(p, o[J * (u >> 1) + j]); else unit_put<0>(p, o[J * (u >> 1) + j]);
      }
    }
}
template <int J> DI void unit_reduce(LAS char* ex, int kg, const f32x16 (&o)[2 * J], float (&v)[J][8]) {
  if (kg == 0) unit_send<0, J>(ex, o, v); else if (kg == 1) unit_send<1, J>(ex, o, v); else if (kg == 2) unit_send<2, J>(ex, o, v); else unit_send<3, J>(ex, o, v);
  WAITL0(); BAR(); SB();
  ex += kg * (3 * J * 2048);
#pragma unroll
  for (int j = 0; j < J; ++j)
#pragma unroll
    for (int s = 0; s < 3; ++s) {
      const f32x4 a = *(LAS const f32x4*)(ex + (s * J + j) * 2048), b = *(LAS const f32x4*)(ex + (s * J + j) * 2048 + 1024);
#pragma unroll
      for (int e = 0; e < 4; ++e) { v[j][e] += a[e]; v[j][4 + e] += b[e]; }
    }
}
__global__ __launch_bounds__(512, 2) void k3_gemm(const u16* __restrict__ wo, const u16* __restrict__ wsp, const u16* __restrict__ bsp, char* __restrict__ ws) {
  extern __shared__ __attribute__((aligned(16))) char lds_[];
  LAS char* lds = (LAS char*)lds_;
  const int tid = threadIdx.x, lane = tid & 63, wid = rfl(tid >> 6), r = lane & 31, h = lane >> 5, q4 = (lane >> 2) & 3;
  const int bid = blockIdx.x, xcd = bid & 7, idx = bid >> 3;
  const unsigned ldsb = (unsigned)(size_t)lds;
  TSD; TS(0);
#define MIX 0
  if (EXPA == 3 && xcd >= 4) return;
  if (EXPA == 4 && xcd < 4) return;
  if (MIX ? idx < 16 : xcd < 4) {
    const int rb = MIX ? (xcd >> 2) * 4 + (idx >> 2) : idx >> 2, nb = idx & 3, kq = xcd & 3;
    GC c; GR g; GNone x;
    c.rA = mkrsrc(ws + WS_C, 16u << 20); c.rB = mkrsrc(wo, 4096 * DM * 2);
    c.vo = lane * 16; c.voB[0] = (lane >> 4) * 1024 + (((lane & 15) ^ ((lane >> 4) << 2)) * 16); c.voB[1] = c.voB[0] + 3072;
    c.soA = ((rb * 8 + wid) * 256 + kq * 64) * 1024; c.soB = (kq * 1024 + wid * 8) * 1024 + nb * 256; c.m0 = ldsb + wid * 2048;
    gc_start<16, 1024>(c, g);
#pragma unroll
    for (int i = 0; i < 4; ++i) c.bR[i] = lds + (4 * h + q4) * 256 + ((i ^ q4) * 64) + ((lane >> 4) & 1) * 32 + (lane & 3) * 8;
    f32x16 o[4] = {};
    WAITV(10); BAR(); SB();
    TS(1);
    gc_first<16, 2048>(c, g);
    gc_run<16, 1024, 2048, 0>(c, g, o, x);
    TS(3);
    const rsrc_t rp = mkrsrc(ws + WS_P, 8u << 20);
    const unsigned so = ((kq * 64 + rb * 8 + wid) * 16 + nb * 4) * 2048;
#pragma unroll
    for (int i = 0; i < 4; ++i) { bst16(pack8<0>(o[i]), rp, c.vo, so + i * 2048); bst16(pack8<1>(o[i]), rp, c.vo, so + i * 2048 + 1024); }
  } else {
    const int rb = MIX ? 2 * xcd + ((idx >> 3) & 1) : (xcd - 4) * 4 + (idx >> 3), nb = idx & 7, wm = wid & 1, kg = wid >> 1;
    const int rb32 = rb * 4 + 2 * wm + (kg >> 1), k16 = 6 * nb + (kg & 1);
    const unsigned zo = (rb32 * 43 + k16) * 1024;
    K3B c;
    c.rA = mkrsrc(wsp, SEQ * SEQ * 2); c.rB = mkrsrc(ws + WS_Z2N, SEQ * ZLD * 2);
#pragma unroll
    for (int i = 0; i < 2; ++i) {
      c.voA[i] = (8 * i + (lane >> 3)) * 4096 + (((lane & 7) ^ (4 * i + (lane >> 4))) * 16) - i * 1024;
      const int o = (2 * wid + i) * 64 + lane;
      c.voB[i] = wid < 6 ? (o / 12) * (ZLD * 2) + (o % 12) * 16 + nb * 192 - i * 1024 : 0x80000000u;
    }
    c.soA = (rb * 128 + wid * 16) * 4096; c.m0 = ldsb + wid * 2048;
    c.rot = ROT & 16 ? nb + 2 * (idx >> 3) : ROT & 32 ? nb : ROT & 128 ? (nb & 3) * 2 : 0;
#define B4(T) k3b_dma<T, 0>(c); k3b_dma<T, 1>(c); k3b_dma<T, 2>(c); k3b_dma<T, 3>(c)
    B4(0); B4(1); if (PB > 2) { B4(2); } if (PB > 3) { B4(3); } if (PB > 4) { B4(4); }
    u32x4 z[3];
    {
      const rsrc_t rz = mkrsrc(ws + WS_Z1, SEQ * FH * 2);
#pragma unroll
      for (int j = 0; j < 3; ++j) asm volatile("buffer_load_dwordx4 %0, %1, %2, %3 offen" : "=v"(z[j]) : "v"(lane * 16), "s"(rz), "s"(zo + j * 2048) : "memory");
    }
    float b;
    asm volatile("buffer_load_ushort %0, %1, %2, %3 offen" : "=v"(b) : "v"(r * 2), "s"(mkrsrc(bsp, SEQ * 2)), "s"(rb32 * 64) : "memory");
    c.aR = lds + (wm * 64 + r) * 128 + ((h ^ ((r >> 1) & 7) ^ (2 * kg)) * 16);
    c.bR = lds + 16384 + (16 * kg + 4 * h + q4) * 192 + ((lane >> 4) & 1) * 32 + (lane & 3) * 8;
    f32x16 o[6] = {};
    K3F f0, f1;
    WAITVL(4 * PB); BAR(); SB();
    TS(1);
    k3b_rd<0, 0>(c, f0); k3b_rd<0, 1>(c, f0); k3b_rd<0, 2>(c, f0); k3b_rd<0, 3>(c, f0); SB();
    k3b_run<0>(c, f0, f1, o);
    TS(3);
    BAR();
    asm volatile("" : "+v"(z[0]), "+v"(z[1]), "+v"(z[2]), "+v"(b));
    b = __uint_as_float(__float_as_uint(b) << 16);
    float v[3][8];
    unit_reduce<3>(lds + wm * 73728 + lane * 16, kg, o, v);
    const rsrc_t rh = mkrsrc(ws + WS_H, SEQ * FH * 2);
#pragma unroll
    for (int j = 0; j < 3; ++j) {
      u32x4 w;
#pragma unroll
      for (int e = 0; e < 4; ++e) w[e] = cvtpk(bflo(z[j][e]) * (v[j][2 * e] + b), bfhi(z[j][e]) * (v[j][2 * e + 1] + b));
      if (k16 + 2 * j < 43) bst16(w, rh, lane * 16, zo + j * 2048);
    }
  }
  TS(4); WAITV(0); TS(5); TSW(3);
}

constexpr int K4_LDS = 90112;
__global__ __launch_bounds__(512, 2) void k4_out(const u16* __restrict__ x, const u16* __restrict__ wout, char* __restrict__ ws, u16* __restrict__ out) {
  extern __shared__ __attribute__((aligned(16))) char lds_[];
  LAS char* lds = (LAS char*)lds_;
  const int tid = threadIdx.x, lane = tid & 63, wid = rfl(tid >> 6), r = lane & 31, h = lane >> 5;
  const int bid = blockIdx.x, rb = (bid & 7) * 4 + ((bid >> 3) & 3), cb = bid >> 5;
  const int wm = wid & 1, kg = wid >> 1, rb32 = rb * 2 + wm;
  TSD; TS(0);
  {
    const rsrc_t rw = mkrsrc(wout, FH * DM * 2);
    const unsigned vo = (lane >> 3) * 1024 + (((lane & 7) ^ (((lane >> 4) & 1) << 2)) * 16), l0 = (unsigned)(size_t)lds + wid * 11264, so = wid * 11 * 8192 + cb * 128;
#pragma unroll
    for (int pc = 0; pc < 11; ++pc) dma16<0>(rw, pc >= 9 && wid == 7 ? 0x80000000u : vo, so + pc * 8192, l0 + pc * 1024);
  }
  u32x4 hf[11], pp[4]; u32x2 xv[2];
  {
    const rsrc_t rh = mkrsrc(ws + WS_H, SEQ * FH * 2), rp = mkrsrc(ws + WS_P, 8u << 20), rx = mkrsrc(x, SEQ * DM * 2);
#pragma unroll
    for (int i = 0; i < 11; ++i)
      asm volatile("buffer_load_dwordx4 %0, %1, %2, %3 offen" : "=v"(hf[i]) : "v"(i == 10 && kg == 3 ? 0x80000000u : lane * 16), "s"(rh), "s"((rb32 * 43 + kg * 11 + i) * 1024) : "memory");
#pragma unroll
    for (int i = 0; i < 4; ++i)
      asm volatile("buffer_load_dwordx4 %0, %1, %2, %3 offen" : "=v"(pp[i]) : "v"(lane * 16), "s"(rp), "s"(((i * 64 + rb32) * 32 + cb * 4 + kg) * 1024) : "memory");
#pragma unroll
    for (int i = 0; i < 2; ++i)
      asm volatile("buffer_load_dwordx2 %0, %1, %2, %3 offen offset:%4" : "=v"(xv[i]) : "v"(r * 1024 + h * 8), "s"(rx), "s"(rb32 * 32768 + cb * 128 + kg * 32), "i"(i * 16) : "memory");
  }
  const int q4 = (lane >> 2) & 3;
  LAS const char* bR[2];
#pragma unroll
  for (int j = 0; j < 2; ++j) bR[j] = lds + kg * 22528 + (4 * h + q4) * 128 + ((j ^ (q4 >> 1)) * 64) + ((lane >> 4) & 1) * 32 + (lane & 3) * 8;
  f32x16 o[2] = {};
  TS(1);
  WAITV(6); BAR(); SB();
  TS(2);
#pragma unroll
  for (int i = 0; i < 11; ++i) asm volatile("" : "+v"(hf[i]));
#pragma unroll
  for (int ks = 0; ks < 11; ++ks)
#pragma unroll
    for (int j = 0; j < 2; ++j) o[j] = MFMA(ldstr(bR[j] + ks * 2048, bR[j] + ks * 2048 + 1024), __builtin_bit_cast(bf16x8, hf[ks]), o[j]);
  TS(3);
  WAITVL(0); BAR(); SB();
  asm volatile("" : "+v"(pp[0]), "+v"(pp[1]), "+v"(pp[2]), "+v"(pp[3]), "+v"(xv[0]), "+v"(xv[1]));
  float v[1][8];
  unit_reduce<1>(lds + wm * 24576 + lane * 16, kg, o, v);
  u32x4 w;
#pragma unroll
  for (int e = 0; e < 4; ++e)
    w[e] = cvtpk(v[0][2 * e] + bflo(xv[e >> 1][e & 1]) + ((bflo(pp[0][e]) + bflo(pp[1][e])) + (bflo(pp[2][e]) + bflo(pp[3][e]))),
                 v[0][2 * e + 1] + bfhi(xv[e >> 1][e & 1]) + ((bfhi(pp[0][e]) + bfhi(pp[1][e])) + (bfhi(pp[2][e]) + bfhi(pp[3][e]))));
  bst16(natural8(w), mkrsrc(out, SEQ * DM * 2), r * 1024 + h * 16, rb32 * 32768 + cb * 128 + kg * 32);
  TS(4); WAITV(0); TS(5); TSW(4);
}

extern "C" void kernel_launch(void* const* d_in, const int* in_sizes, int n_in, void* d_out, int out_size, void* d_ws, size_t ws_size, hipStream_t stream) {
  (void)in_sizes; (void)n_in; (void)out_size; (void)ws_size;
  const u16* x = (const u16*)d_in[0]; const u16* ckv = (const u16*)d_in[1]; const u16* wq = (const u16*)d_in[2]; const u16* wk = (const u16*)d_in[3];
  const u16* wv = (const u16*)d_in[4]; const u16* wo = (const u16*)d_in[5]; const u16* w_in = (const u16*)d_in[7]; const u16* wsp = (const u16*)d_in[8];
  const u16* bsp = (const u16*)d_in[9]; const u16* wout = (const u16*)d_in[10];
  char* ws = (char*)d_ws;
  (void)hipFuncSetAttribute((const void*)k1_kvz, hipFuncAttributeMaxDynamicSharedMemorySize, K1_LDS);
  (void)hipFuncSetAttribute((const void*)k2_attn, hipFuncAttributeMaxDynamicSharedMemorySize, K2_LDS);
  (void)hipFuncSetAttribute((const void*)k3_gemm, hipFuncAttributeMaxDynamicSharedMemorySize, K3_LDS);
  (void)hipFuncSetAttribute((const void*)k4_out, hipFuncAttributeMaxDynamicSharedMemorySize, K4_LDS);
  k1_kvz<<<256, 512, K1_LDS, stream>>>(x, ckv, wk, wv, w_in, ws, wq, wo, wsp, wout);
  k2_attn<<<256, 512, K2_LDS, stream>>>(wq, ws);
  k3_gemm<<<256, 512, K3_LDS, stream>>>(wo, wsp, bsp, ws);
  k4_out<<<256, 512, K4_LDS, stream>>>(x, wout, ws, (u16*)d_out);
}
```

```cpp
#include <hip/hip_runtime.h>
#include <hip/hip_bf16.h>
#include <stdint.h>

typedef __bf16 bf16x8 __attribute__((ext_vector_type(8)));
typedef __bf16 bf16x4 __attribute__((ext_vector_type(4)));
typedef __bf16 bf16x2 __attribute__((ext_vector_type(2)));
typedef float f32x16 __attribute__((ext_vector_type(16)));
typedef float f32x4 __attribute__((ext_vector_type(4)));
typedef float f32x2 __attribute__((ext_vector_type(2)));
typedef unsigned u32x4 __attribute__((ext_vector_type(4)));
typedef unsigned u32x2 __attribute__((ext_vector_type(2)));
typedef unsigned short u16;
typedef __amdgpu_buffer_rsrc_t rsrc_t;
#define LAS __attribute__((address_space(3)))
#define DI __device__ __forceinline__
#define SB() __builtin_amdgcn_sched_barrier(0)
#define MFMA(a, b, c) __builtin_amdgcn_mfma_f32_32x32x16_bf16((a), (b), (c), 0, 0, 0)
#define WAITV(n) asm volatile("s_waitcnt vmcnt(" #n ")" ::: "memory")
#define WAITL0() asm volatile("s_waitcnt lgkmcnt(0)" ::: "memory")
#define BAR() do { asm volatile("" ::: "memory"); __builtin_amdgcn_s_barrier(); asm volatile("" ::: "memory"); } while (0)

#define TSD
#define TSR
#define TS(i)
#define TSW(k)
constexpr int SEQ = 2048, DM = 512, NH = 32, HD = 128, FH = 688, FI = 1376, ZLD = 768;
constexpr size_t WS_XN = 0;
constexpr size_t WS_Z1 = WS_XN + (size_t)SEQ * DM * 2;
constexpr size_t WS_Z2 = WS_Z1 + (size_t)SEQ * FH * 2;
constexpr size_t WS_Z2N = WS_Z2 + (size_t)SEQ * FH * 2;
constexpr size_t WS_H = WS_Z2N + (size_t)SEQ * ZLD * 2;
constexpr size_t WS_K = 48u << 20;
constexpr size_t WS_V = WS_K + (16u << 20);
constexpr size_t WS_C = WS_V + (16u << 20);
constexpr size_t WS_P = WS_C + (16u << 20);
constexpr size_t WS_F = 120u << 20;
static_assert(WS_H + (size_t)SEQ * FH * 2 <= WS_K, "ws map");

DI unsigned rfl(unsigned v) { return __builtin_amdgcn_readfirstlane(v); }
DI unsigned cvtpk(float lo, float hi) { f32x2 v = {lo, hi}; bf16x2 b = __builtin_convertvector(v, bf16x2); return __builtin_bit_cast(unsigned, b); }
DI float bflo(unsigned u) { return __uint_as_float(u << 16); }
DI float bfhi(unsigned u) { return __uint_as_float(u & 0xffff0000u); }
DI float rbf(float v) { return bflo(cvtpk(v, v) & 0xffffu); }
template <int S> DI u32x4 pack8(const f32x16& c) {
  u32x4 w = {cvtpk(c[8 * S + 0], c[8 * S + 1]), cvtpk(c[8 * S + 2], c[8 * S + 3]), cvtpk(c[8 * S + 4], c[8 * S + 5]), cvtpk(c[8 * S + 6], c[8 * S + 7])};
  return w;
}
DI rsrc_t mkrsrc(const void* p, unsigned bytes) { return __builtin_amdgcn_make_buffer_rsrc((void*)p, (short)0, (int)bytes, 0x00020000); }
template <int IMM> DI void dma16(rsrc_t r, unsigned voff, unsigned soff, unsigned m0v) {
  asm volatile("s_mov_b32 m0, %0\n\ts_nop 0\n\tbuffer_load_dwordx4 %1, %2, %3 offen offset:%4 lds" ::"s"(m0v), "v"(voff), "s"(r), "s"(soff), "i"(IMM) : "memory");
}
DI bf16x8 ldsr(LAS const char* p) { return *(LAS const bf16x8*)p; }
DI bf16x8 ldstr(LAS const char* p0, LAS const char* p1) {
  bf16x4 a = __builtin_amdgcn_ds_read_tr16_b64_v4bf16((LAS bf16x4*)p0);
  bf16x4 b = __builtin_amdgcn_ds_read_tr16_b64_v4bf16((LAS bf16x4*)p1);
  return __builtin_shufflevector(a, b, 0, 1, 2, 3, 4, 5, 6, 7);
}
DI float swap32_add(float v) { auto r = __builtin_amdgcn_permlane32_swap(__float_as_uint(v), __float_as_uint(v), false, false); return __uint_as_float(r[0]) + __uint_as_float(r[1]); }
DI float swap32_max(float v) { auto r = __builtin_amdgcn_permlane32_swap(__float_as_uint(v), __float_as_uint(v), false, false); return fmaxf(__uint_as_float(r[0]), __uint_as_float(r[1])); }
DI u32x4 natural8(u32x4 w) {
  const auto a = __builtin_amdgcn_permlane32_swap(w[0], w[2], false, false), b = __builtin_amdgcn_permlane32_swap(w[1], w[3], false, false);
  return (u32x4){a[0], b[0], a[1], b[1]};
}
template <int IMM> DI void dma16n(rsrc_t r, unsigned voff, unsigned soff) {
  asm volatile("buffer_load_dwordx4 %0, %1, %2 offen offset:%3 lds" ::"v"(voff), "s"(r), "s"(soff), "i"(IMM) : "memory");
}
template <int AUX = 16> DI void bst16(u32x4 v, rsrc_t r, unsigned vo, unsigned so) { __builtin_amdgcn_raw_buffer_store_b128(v, r, vo, so, AUX); }
#define WAITVL(n) asm volatile("s_waitcnt vmcnt(%0) lgkmcnt(0)" ::"i"(n) : "memory")
DI void ln_stats(float s, float q, float invn, float& mb, float& rs) {
  const float mean = s * invn, var = fmaxf(fmaf(-mean, mean, q * invn), 0.f);
  mb = rbf(mean); rs = 1.f / rbf(sqrtf(rbf(rbf(var) + 1e-5f)));
}
DI float dot2(unsigned a, unsigned b, float c) { return __builtin_amdgcn_fdot2_f32_bf16(__builtin_bit_cast(bf16x2, a), __builtin_bit_cast(bf16x2, b), c, false); }
enum { F_XN = 4096, F_KV = 8192, F_ZN = 12288, F_Z = 16384, F_CTX = 65536, F_P = 131072 };
struct FL { rsrc_t r; unsigned lo, hi; };
struct Args { const u16 *x, *ckv, *wq, *wk, *wv, *wo, *w_in, *wsp, *bsp, *wout; char* ws; u16* out; };
typedef __attribute__((address_space(4))) const Args* KA;
DI KA kargs() { KA a = (KA)__builtin_amdgcn_kernarg_segment_ptr(); asm volatile("" : "+s"(a)); return a; }
DI void fl_pub(const FL& f, unsigned off, bool lead) { if (lead) __builtin_amdgcn_raw_buffer_store_b64((u32x2){f.lo, f.hi}, f.r, 0, off, 16); }
DI void fl_ld(const FL& f, u32x2& v, unsigned vo) { asm volatile("buffer_load_dwordx2 %0, %1, %2, 0 offen sc1" : "=v"(v) : "v"(vo), "s"(f.r) : "memory"); }
template <bool ACQ> DI void fl_chk(const FL& f, u32x2 v, unsigned vo) {
  asm volatile("" : "+v"(v));
#pragma unroll 1
  for (int i = 0; i < (1 << 18) && __any(v[0] != f.lo || v[1] != f.hi); ++i) {
    __builtin_amdgcn_s_sleep(2); fl_ld(f, v, vo);
    asm volatile("s_waitcnt vmcnt(0)" : "+v"(v)::"memory");
  }
  if (ACQ) asm volatile("buffer_inv sc1" ::: "memory");
}
struct GC { rsrc_t rA, rB; unsigned vo, voB[2], soA, soB, m0; LAS const char* bR[4]; };
struct GR { u32x4 a[3][4]; bf16x8 w[2][4]; };
template <int NP, int LDB, int T, int P> DI void gc_dma(const GC& c) {
  if constexpr (T < NP) { if constexpr (P == 0) dma16<0>(c.rB, c.voB[0], c.soB + T * 64 * LDB, c.m0 + (T & 3) * 16384); else dma16n<1024>(c.rB, c.voB[1], c.soB + T * 64 * LDB); }
}
template <int NP, int T, int KS> DI void gc_lda(const GC& c, GR& g) {
  if constexpr (T < NP) asm volatile("buffer_load_dwordx4 %0, %1, %2, %3 offen offset:%4 sc1" : "=v"(g.a[T % 3][KS]) : "v"(c.vo), "s"(c.rA), "s"(c.soA + T * 4096), "i"(KS * 1024) : "memory");
}
template <int NP, int HO, int N, int J> DI void gc_rd(const GC& c, GR& g) {
  if constexpr (N < 4 * NP) { LAS const char* b = c.bR[J] + ((N >> 2) & 3) * 16384 + (N & 3) * 4096; g.w[N & 1][J] = ldstr(b, b + HO); }
}
template <int NP, int LDB, int HO, int N, class X> DI void gc_step(const GC& c, GR& g, f32x16 (&o)[4], X& x) {
  constexpr int T = N >> 2, KS = N & 3;
  if constexpr (KS == 3 && T + 1 < NP) {
    x.template pre<T>();
    WAITVL((T > 0 && T + 3 < NP ? 2 : 0) + (T + 2 < NP ? 4 : 0) + X::extra(T)); BAR();
    asm volatile("" : "+v"(g.a[(T + 1) % 3][0]), "+v"(g.a[(T + 1) % 3][1]), "+v"(g.a[(T + 1) % 3][2]), "+v"(g.a[(T + 1) % 3][3])); SB();
  }
  const bf16x8 a = __builtin_bit_cast(bf16x8, g.a[T % 3][KS]);
  o[0] = MFMA(g.w[N & 1][0], a, o[0]); SB(); gc_rd<NP, HO, N + 1, 0>(c, g); SB();
  o[1] = MFMA(g.w[N & 1][1], a, o[1]); SB(); gc_rd<NP, HO, N + 1, 1>(c, g);
  if constexpr (KS == 3) gc_dma<NP, LDB, T + 4, 0>(c); else gc_lda<NP, T + 2, KS>(c, g);
  SB();
  o[2] = MFMA(g.w[N & 1][2], a, o[2]); SB(); gc_rd<NP, HO, N + 1, 2>(c, g); x.template at<N>(); SB();
  o[3] = MFMA(g.w[N & 1][3], a, o[3]); SB(); gc_rd<NP, HO, N + 1, 3>(c, g);
  if constexpr (KS == 3) gc_dma<NP, LDB, T + 4, 1>(c); else if constexpr (KS == 2) gc_lda<NP, T + 2, 3>(c, g);
  SB();
}
template <int NP, int LDB, int HO, int N, class X> DI void gc_run(const GC& c, GR& g, f32x16 (&o)[4], X& x) {
  if constexpr (N < 4 * NP) { gc_step<NP, LDB, HO, N>(c, g, o, x); gc_run<NP, LDB, HO, N + 1>(c, g, o, x); }
}
template <int NP, int LDB> DI void gc_dma4(const GC& c) {
  gc_dma<NP, LDB, 0, 0>(c); gc_dma<NP, LDB, 0, 1>(c); gc_dma<NP, LDB, 1, 0>(c); gc_dma<NP, LDB, 1, 1>(c);
  gc_dma<NP, LDB, 2, 0>(c); gc_dma<NP, LDB, 2, 1>(c); gc_dma<NP, LDB, 3, 0>(c); gc_dma<NP, LDB, 3, 1>(c);
}
template <int NP> DI void gc_lda2(const GC& c, GR& g) {
  gc_lda<NP, 0, 0>(c, g); gc_lda<NP, 0, 1>(c, g); gc_lda<NP, 0, 2>(c, g); gc_lda<NP, 0, 3>(c, g);
  gc_lda<NP, 1, 0>(c, g); gc_lda<NP, 1, 1>(c, g); gc_lda<NP, 1, 2>(c, g); gc_lda<NP, 1, 3>(c, g);
}
template <int NP, int HO> DI void gc_first(const GC& c, GR& g) {
  asm volatile("" : "+v"(g.a[0][0]), "+v"(g.a[0][1]), "+v"(g.a[0][2]), "+v"(g.a[0][3]));
  gc_rd<NP, HO, 0, 0>(c, g); gc_rd<NP, HO, 0, 1>(c, g); gc_rd<NP, HO, 0, 2>(c, g); gc_rd<NP, HO, 0, 3>(c, g); SB();
}

constexpr int LDSZ = 163840;
constexpr float QSCALE = 0.08838834764831845f * 1.4426950408889634f;
struct K1C {
  rsrc_t rA, rW0, rW1, rX, rI, rK;
  unsigned voA[2], voB, voI[2], vo, m0A, m0B, soA, soB, soX, soK; int rot;
  LAS const char* aR[4]; LAS const char* bR;
  rsrc_t rP, rN; unsigned voP, soP, voN, soN, voF; u32x4 xn; FL fl;
};
#define PFK 21
constexpr int NPRO = 10, QUO = 4;
struct K1Op { int kind, item, piece; };
constexpr int k1_xslot(int i) { return i % 5 == 0 ? 6 : i % 5 == 1 ? 7 : i == 2 ? 4 : i == 3 ? 5 : i == 4 ? 9 : 4; }
constexpr int k1_wslot(int i) { return i % 5 == 0 ? 2 : i % 5 == 1 ? 3 : i == 3 ? 0 : i == 4 ? 1 : 8; }
constexpr K1Op k1_free(int p, int i) {
  if (p < 0) { const int q = p + 10; constexpr int it[10] = {0, 0, 1, 1, 2, 3, 2, 4, 5, 3}; return i < 2 ? K1Op{q % 3 == 0 ? 2 : 1, it[q], i} : K1Op{0, 0, 0}; }
  if (i < 2) return p < 26 ? K1Op{1, p + 6, i} : p == 26 ? K1Op{4, 0, i} : p == 27 ? K1Op{4, 1, i} : p == 28 ? K1Op{3, 2, i} : p == 29 ? K1Op{3, 3, i} : p == 30 ? K1Op{4, 3, i}
                  : p == 31 ? K1Op{4, 4, i} : p < 35 ? K1Op{3, p - 27, i} : K1Op{0, 0, 0};
  if (i == 2) return p == 0 ? K1Op{0, 0, 0} : p < 25 ? K1Op{2, (p - 1) / 2 + 4, (p - 1) & 1} : p < 29 ? K1Op{3, (p - 25) / 2, (p - 1) & 1} : p < 31 ? K1Op{4, 2, (p - 1) & 1}
                   : p == 31 ? K1Op{3, 4, 0} : p < 35 ? K1Op{4, p - 27, 0} : K1Op{0, 0, 0};
  return p == 31 ? K1Op{3, 4, 1} : p >= 32 && p < 35 ? K1Op{4, p - 27, 1} : K1Op{0, 0, 0};
}
constexpr int k1_cum(int p) {
  int n = 0;
  for (int q = -10; q <= p; ++q) for (int i = 0; i < 4; ++i) n += k1_free(q, i).kind != 0;
  return p < -1 ? 0 : n < NPRO + QUO * (p + 1) ? n : NPRO + QUO * (p + 1);
}
constexpr K1Op k1_seq(int n) {
  for (int q = -10; q < 40; ++q) for (int i = 0; i < 4; ++i) if (k1_free(q, i).kind && n-- == 0) return k1_free(q, i);
  return K1Op{0, 0, 0};
}
constexpr K1Op k1_op(int p, int i) {
  if (p < 0 || i < 4) return k1_cum(p - 1) + i < k1_cum(p) ? k1_seq(k1_cum(p - 1) + i) : K1Op{0, 0, 0};
  if (i == 4) return p == 0 ? K1Op{7, 0, 0} : p >= 16 && p < 31 && !(p & 1) ? K1Op{5, 0, (p - 16) / 2} : p == 32 || p == 33 ? K1Op{5, 1, p - 32} : p >= 34 && p < 37 ? K1Op{5, 1, 2 * p - 66} : K1Op{0, 0, 0};
  return p >= 34 && p < 37 ? K1Op{5, 1, 2 * p - 65} : p == 17 ? K1Op{8, 0, 0} : p == PFK ? K1Op{6, 0, 0} : K1Op{0, 0, 0};
}
constexpr bool k1_needs(int p, K1Op o) {
  return p < 32 ? (o.kind == 1 && o.item == p) || (o.kind == 2 && o.item == p / 2) : (o.kind == 3 || o.kind == 4) && o.item == p - 32;
}
constexpr int k1_wait(int p, int upto) {
  int n = 0, got = 0;
  for (int q = -1; q < upto; ++q) for (int i = 0; i < (q < 0 ? 20 : 6); ++i) { const K1Op o = k1_op(q, i); if (k1_needs(p, o)) { n = 0; ++got; } else if (o.kind) ++n; }
  return got == 4 ? n : -1;
}
struct K1P { unsigned t; u32x2 f; };
struct K1A { f32x16 k[2], v[2]; };
template <int P, int I> DI void k1_vm(const K1C& c, const K1A& a0, const K1A& a1, K1P& pf) {
  constexpr K1Op o = k1_op(P, I);
  if constexpr (o.kind == 1) dma16<0>(o.piece ? c.rW1 : c.rW0, c.voB, c.soB + ((o.item + 2 * c.rot) & 15) * 262144, c.m0B + (o.item % 6) * 16384 + o.piece * 8192);
  if constexpr (o.kind == 2) dma16<0>(c.rA, c.voA[o.piece], c.soA + (o.item >> 3) * 131072 + ((o.item + c.rot) & 7) * 128, c.m0A + (6 + (o.item & 3)) * 16384 + o.piece * 1024);
  if constexpr (o.kind == 3) dma16<0>(c.rX, c.voA[o.piece], c.soX + ((o.item + c.rot) & 7) * 128, c.m0A + k1_xslot(o.item) * 16384 + o.piece * 1024);
  if constexpr (o.kind == 4) dma16<0>(c.rI, c.voI[o.piece], ((o.item + c.rot) & 7) * (64 * FI * 2), c.m0A + k1_wslot(o.item) * 16384 + o.piece * 1024);
  if constexpr (o.kind == 5) {
    const K1A& a = o.item ? a1 : a0;
    const f32x16& t = o.piece & 2 ? a.v[o.piece >> 2] : a.k[o.piece >> 2];
    bst16(pack8<o.piece & 1>(t), c.rK, c.vo, c.soK + ((o.piece >> 1) & 1) * (16u << 20) + o.item * 32768 + (o.piece >> 2) * 8192 + (o.piece & 1) * 1024);
  }
  if constexpr (o.kind == 7) bst16(c.xn, c.rN, c.voN, c.soN);
  if constexpr (o.kind == 8) fl_ld(c.fl, pf.f, c.voF);
  if constexpr (o.kind == 6) asm volatile("buffer_load_dword %0, %1, %2, %3 offen" : "+v"(pf.t) : "v"(c.voP), "s"(c.rP), "s"(c.soP) : "memory");
}
struct K1F { bf16x8 a[2][2], w[2][2]; };
template <int P, int G> DI void k1_rd(const K1C& c, K1F& f) {
  if constexpr (P < 32) {
    constexpr int KS = G / 3;
    if constexpr (G % 3 == 1) { LAS const char* a = c.aR[2 * (P & 1) + KS] + ((P >> 1) & 3) * 16384; f.a[KS][0] = ldsr(a); f.a[KS][1] = ldsr(a + 4096); }
    else { LAS const char* b = c.bR + (P % 6) * 16384 + KS * 4096 + (G % 3 / 2) * 8192; f.w[KS][G % 3 / 2] = ldstr(b, b + 1024); }
  }
}
struct K1Z { bf16x8 x[2], w[2][3], d[2]; };
struct K1ZA { LAS const char* xR[2]; LAS const char* wR; LAS const char* dR; f32x16 acc[3]; float s, q, cs; };
template <int S, int K, int G> DI void k1z_rd(const K1ZA& z, K1Z& f) {
  if constexpr (S < 8) {
    constexpr int o = k1_wslot(S) * 16384 + K * 16 * 192;
    f.w[K][G] = ldstr(z.wR + o + G * 64, z.wR + o + G * 64 + 768);
    if constexpr (G == 0) f.x[K] = ldsr(z.xR[K] + k1_xslot(S) * 16384);
    if constexpr (G == 1) f.d[K] = ldstr(z.dR + o, z.dR + o + 768);
  }
}
template <int P> DI void k1_per(const K1C& c, const K1F& f, K1F& fn, K1A& a0, K1A& a1, K1P& pf, const K1ZA& z, K1Z& g) {
  K1A& a = P < 16 ? a0 : a1;
  static_assert(k1_wait(P + 1, P) >= 0, "operand not requested");
  WAITVL(k1_wait(P + 1, P)); BAR(); SB();
#define KM(ks, i) a.k[i] = MFMA(f.w[ks][0], f.a[ks][i], a.k[i]); SB()
#define VM(ks, i) a.v[i] = MFMA(f.a[ks][i], f.w[ks][1], a.v[i]); SB()
#define KR(G) if constexpr (P < 31) k1_rd<P + 1, G>(c, fn); else k1z_rd<0, (G) / 3, (G) % 3>(z, g)
  if constexpr (P == 8) fl_pub(c.fl, F_XN + blockIdx.x * 8, c.soB < 32768 && c.vo == 0);
  KM(0, 0); KR(0); SB(); KM(0, 1); KR(1); SB(); VM(0, 0); KR(2); SB();
  VM(0, 1); KR(3); k1_vm<P, 0>(c, a0, a1, pf); SB(); KM(1, 0); KR(4); k1_vm<P, 1>(c, a0, a1, pf); SB(); KM(1, 1); KR(5); k1_vm<P, 2>(c, a0, a1, pf); SB();
  VM(1, 0); k1_vm<P, 3>(c, a0, a1, pf); SB(); VM(1, 1); k1_vm<P, 4>(c, a0, a1, pf); k1_vm<P, 5>(c, a0, a1, pf);
  if constexpr (P == 24) { if (c.soB < 32768) fl_chk<false>(c.fl, pf.f, c.voF); }
  SB();
}
template <int P, int E> DI void k1_run(const K1C& c, K1F& f0, K1F& f1, K1A& a0, K1A& a1, K1P& pf, const K1ZA& z, K1Z& g) {
  if constexpr (P < E) { k1_per<P>(c, f0, f1, a0, a1, pf, z, g); k1_per<P + 1>(c, f1, f0, a0, a1, pf, z, g); k1_run<P + 2, E>(c, f0, f1, a0, a1, pf, z, g); }
}
template <int S, int K> DI void k1z_half(const K1C& c, const K1A& a1, K1ZA& z, const K1Z& f, K1Z& fn, K1P& pf) {
  const u32x4 x = __builtin_bit_cast(u32x4, f.x[K]), d = __builtin_bit_cast(u32x4, f.d[K]);
  z.acc[0] = MFMA(f.w[K][0], f.x[K], z.acc[0]); SB(); k1z_rd<S + 1, K, 0>(z, fn);
#pragma unroll
  for (int e = 0; e < 4; ++e) z.s = dot2(x[e], 0x3f803f80u, z.s);
  k1_vm<32 + S, 3 * K>(c, a1, a1, pf); SB();
  z.acc[1] = MFMA(f.w[K][1], f.x[K], z.acc[1]); SB(); k1z_rd<S + 1, K, 1>(z, fn);
#pragma unroll
  for (int e = 0; e < 4; ++e) z.q = dot2(x[e], x[e], z.q);
  k1_vm<32 + S, 3 * K + 1>(c, a1, a1, pf); SB();
  z.acc[2] = MFMA(f.w[K][2], f.x[K], z.acc[2]); SB(); k1z_rd<S + 1, K, 2>(z, fn);
#pragma unroll
  for (int e = 0; e < 4; ++e) z.cs = dot2(d[e], 0x3f803f80u, z.cs);
  k1_vm<32 + S, 3 * K + 2>(c, a1, a1, pf); SB();
}
template <int S> DI void k1z_stage(const K1C& c, const K1A& a1, K1ZA& z, const K1Z& f, K1Z& fn, K1P& pf) {
  if constexpr (S < 7) { WAITVL(k1_wait(33 + S, 32 + S)); BAR(); SB(); }
  k1z_half<S, 0>(c, a1, z, f, fn, pf); k1z_half<S, 1>(c, a1, z, f, fn, pf);
}
template <int KH> DI void k1z_send(LAS f32x4* ex, const f32x16 (&acc)[3], f32x4 (&own)[3][2]) {
#pragma unroll
  for (int nb = 0; nb < 3; ++nb)
#pragma unroll
    for (int e = 0; e < 2; ++e) {
      constexpr int o = 8 * KH, t = 8 - o;
      ex[(nb * 2 + e) * 64] = (f32x4){acc[nb][t + 4 * e], acc[nb][t + 4 * e + 1], acc[nb][t + 4 * e + 2], acc[nb][t + 4 * e + 3]};
      own[nb][e] = (f32x4){acc[nb][o + 4 * e], acc[nb][o + 4 * e + 1], acc[nb][o + 4 * e + 2], acc[nb][o + 4 * e + 3]};
      asm volatile("" : "+v"(own[nb][e]));
    }
}
DI void p1_kvz(LAS char* lds, FL& fl, GC& q, GR& qr) {
  const KA ka = kargs();
  char* ws = ka->ws;
  const int tid = threadIdx.x, lane = tid & 63, wid = rfl(tid >> 6), r = lane & 31, h = lane >> 5;
  const int bid = blockIdx.x, xcd = bid & 7, idx = bid >> 3, head = xcd * 4 + (idx >> 3), rb = idx & 7;
  const int wm = wid >> 2, wn = wid & 3, q4 = (lane >> 2) & 3, tt = h ^ ((r >> 1) & 7);
  const int zrb = (xcd >> 1) * 4 + (idx >> 3), cg = (xcd & 1) * 8 + rb;
  TSD; TS(0);
  K1C c;
  K1A a0 = {}, a1 = {};
  K1P pf = {};
  const unsigned ldsb = (unsigned)(size_t)lds;
  u32x4 xv;
  c.rX = mkrsrc(ka->x, SEQ * DM * 2);
  asm volatile("buffer_load_dwordx4 %0, %1, %2, %3 offen" : "=v"(xv) : "v"(lane * 16), "s"(c.rX), "s"((bid * 8 + wid) * 1024) : "memory");
  c.rA = mkrsrc(ka->ckv, SEQ * DM * 2); c.rW0 = mkrsrc(ka->wk, DM * 4096 * 2); c.rW1 = mkrsrc(ka->wv, DM * 4096 * 2);
#pragma unroll
  for (int p = 0; p < 2; ++p) c.voA[p] = (8 * p + (lane >> 3)) * 1024 + (((lane & 7) ^ (4 * p + (lane >> 4))) * 16);
  c.voB = (lane >> 4) * 8192 + (((lane & 15) ^ ((lane >> 4) << 2)) * 16);
  c.soA = (rb * 256 + wid * 16) * 1024; c.soB = wid * 4 * 8192 + head * 256;
  c.rot = (rb + 2 * (idx >> 3)) & 7;
  c.m0A = ldsb + wid * 2048; c.m0B = ldsb + wid * 1024;
#define P2(q) k1_vm<-1, q>(c, a0, a1, pf); k1_vm<-1, q + 1>(c, a0, a1, pf)
  P2(0); P2(2); P2(4); P2(6); P2(8); P2(10); P2(12); P2(14); P2(16); P2(18);
#pragma unroll
  for (int ks = 0; ks < 4; ++ks) c.aR[ks] = lds + 98304 + (wm * 64 + r) * 128 + ((tt ^ (2 * ks)) * 16);
  c.bR = lds + (8 * h + q4) * 256 + ((wn ^ q4) * 64) + ((lane >> 4) & 1) * 32 + (lane & 3) * 8;
  c.rI = mkrsrc(ka->w_in, DM * FI * 2); c.rK = mkrsrc(ws + WS_K, 32u << 20);
  c.soX = (zrb * 128 + wid * 16) * 1024; c.vo = lane * 16;
  c.soK = ((head * 64 + rb * 8 + wm * 2) * 8 + 2 * wn) * 1024;
  c.voF = F_XN + ((lane & 16 ? rb : 15 - rb) * 16 + (lane & 15)) * 8;
#pragma unroll
  for (int i = 0; i < 2; ++i) {
    const int o = (wid * 2 + i) * 64 + lane;
    c.voI[i] = wid < 6 && cg < 15 ? (o / 12) * (FI * 2) + (o % 12) * 16 + cg * 192 : 0x80000000u;
  }
  c.rP = mkrsrc(ka->wq, DM * 4096 * 2); c.voP = r * 64; c.soP = (bid * 8 + wid) * 2048;
  const int wr = wid & 3, kh = wid >> 2;
  K1ZA z; K1Z g0, g1;
#pragma unroll
  for (int k = 0; k < 2; ++k) z.xR[k] = lds + (wr * 32 + r) * 128 + ((tt ^ (2 * (2 * kh + k))) * 16);
  z.wR = lds + (32 * kh + 8 * h + q4) * 192 + ((lane >> 4) & 1) * 32 + (lane & 3) * 8;
  z.dR = z.wR + (wr % 3) * 64;
  unsigned long long ep;
  asm volatile("s_load_dwordx2 %0, %1, 0" : "=s"(ep) : "s"(ws + WS_F) : "memory");
  TS(1);
  WAITVL(NPRO); SB();
  asm volatile("" : "+s"(ep));
  fl.r = mkrsrc(ws + WS_F, 262144); fl.lo = (unsigned)(ep + 1); fl.hi = (unsigned)((ep + 1) >> 32); c.fl = fl;
  {
    asm volatile("" : "+v"(xv));
    float s = 0.f, q = 0.f;
#pragma unroll
    for (int e = 0; e < 4; ++e) { s = dot2(xv[e], 0x3f803f80u, s); q = dot2(xv[e], xv[e], q); }
#pragma unroll
    for (int o = 1; o < 32; o <<= 1) { s += __shfl_xor(s, o); q += __shfl_xor(q, o); }
    s = swap32_add(s); q = swap32_add(q);
    float mb, rs; ln_stats(s, q, 1.f / DM, mb, rs);
#pragma unroll
    for (int e = 0; e < 4; ++e) c.xn[e] = cvtpk((bflo(xv[e]) - mb) * rs, (bfhi(xv[e]) - mb) * rs);
    *(LAS u32x4*)(lds + 147456 + lane * 128 + wid * 16) = c.xn;
    static_assert(k1_seq(18).kind == 2 && k1_seq(18).item == 3 && k1_cum(0) <= 18, "slot 9");
    c.rN = mkrsrc(ws + WS_XN, SEQ * DM * 2); c.voN = (lane >> 4) * 1024 + ((lane >> 3) & 1) * 512 + (lane & 7) * 16; c.soN = (bid >> 2) * 32768 + wid * 4096 + (bid & 3) * 128;
  }
  K1F f0, f1;
  SB(); WAITVL(k1_wait(0, 0)); BAR(); SB();
  TS(2);
  c.xn = *(LAS const u32x4*)(lds + 147456 + wid * 1024 + lane * 16);
  k1_rd<0, 0>(c, f0); k1_rd<0, 1>(c, f0); k1_rd<0, 2>(c, f0); k1_rd<0, 3>(c, f0); k1_rd<0, 4>(c, f0); k1_rd<0, 5>(c, f0); SB();
  k1_run<0, 8>(c, f0, f1, a0, a1, pf, z, g0);
  TS(3);
  k1_run<8, 16>(c, f0, f1, a0, a1, pf, z, g0);
  TS(4);
  k1_run<16, 24>(c, f0, f1, a0, a1, pf, z, g0);
  TS(5);
  k1_run<24, 32>(c, f0, f1, a0, a1, pf, z, g0);
  TS(6);
  z.acc[0] = (f32x16){}; z.acc[1] = (f32x16){}; z.acc[2] = (f32x16){}; z.s = 0.f; z.q = 0.f; z.cs = 0.f;
  k1z_stage<0>(c, a1, z, g0, g1, pf); k1z_stage<1>(c, a1, z, g1, g0, pf); k1z_stage<2>(c, a1, z, g0, g1, pf); k1z_stage<3>(c, a1, z, g1, g0, pf);
  k1z_stage<4>(c, a1, z, g0, g1, pf); k1z_stage<5>(c, a1, z, g1, g0, pf); k1z_stage<6>(c, a1, z, g0, g1, pf); k1z_stage<7>(c, a1, z, g1, g0, pf);
  TS(7);
  {
    LAS f32x4* ex = (LAS f32x4*)lds;
    LAS f32x2* st = (LAS f32x2*)(lds + 49152);
    LAS float* cs = (LAS float*)(lds + 51200);
    f32x4 own[3][2];
    if (kh) k1z_send<1>(ex + wid * 384 + lane, z.acc, own); else k1z_send<0>(ex + wid * 384 + lane, z.acc, own);
    { const float s = swap32_add(z.s), q = swap32_add(z.q), cc = swap32_add(z.cs); st[wid * 32 + r] = (f32x2){s, q}; cs[wid * 32 + r] = cc; }
    WAITVL(0); BAR(); SB();
    fl_pub(fl, F_KV + bid * 8, tid == 0);
    q.rA = mkrsrc(ws + WS_XN, SEQ * DM * 2); q.rB = mkrsrc(kargs()->wq, DM * 4096 * 2);
    q.vo = c.vo; q.voB[0] = c.voB; q.voB[1] = c.voB + 31744;
    q.soA = ((kh ? rb : 15 - rb) * 4 + wr) * 32768; q.soB = wid * 65536 + head * 256; q.m0 = ldsb + 65536 + wid * 2048;
    gc_dma4<8, 8192>(q); gc_lda2<8>(q, qr);
    SB();
    const int pw = wid ^ 4, rb32 = zrb * 4 + wr;
    const f32x2 s0 = st[wr * 32 + r], s1 = st[(wr + 4) * 32 + r];
    float mb, rs; ln_stats(s0[0] + s1[0], s0[1] + s1[1], 1.f / DM, mb, rs);
    const float mrs = -mb * rs;
    const rsrc_t rz = mkrsrc(ws + WS_Z1, 2 * SEQ * FH * 2);
#pragma unroll
    for (int nb = 0; nb < 3; ++nb) {
      u32x4 w;
#pragma unroll
      for (int e = 0; e < 2; ++e) {
        const f32x4 c0 = *(LAS const f32x4*)(cs + nb * 32 + 16 * kh + 8 * e + 4 * h), c1 = *(LAS const f32x4*)(cs + 128 + nb * 32 + 16 * kh + 8 * e + 4 * h);
        const f32x4 t = (ex[((pw * 3 + nb) * 2 + e) * 64 + lane] + own[nb][e]) * rs + (c0 + c1) * mrs;
        const f32x4 u = t * (t * t * -0.1029432397f - 2.302208198f);
        f32x4 d;
#pragma unroll
        for (int cc = 0; cc < 4; ++cc) d[cc] = __builtin_amdgcn_exp2f(u[cc]);
        d += 1.f;
#pragma unroll
        for (int cc = 0; cc < 4; ++cc) d[cc] = __builtin_amdgcn_rcpf(d[cc]);
        d *= t;
        w[2 * e] = cvtpk(d[0], d[1]); w[2 * e + 1] = cvtpk(d[2], d[3]);
      }
      const int k16 = 2 * (3 * cg + nb) + kh;
      if (k16 < 43) bst16(w, rz, lane * 16, (rb32 * 43 + k16) * 1024);
      else bst16(natural8(w), rz, k16 < 86 ? lane * 16 : 0x80000000u, SEQ * FH * 2 + (rb32 * 43 + k16 - 43) * 1024);
    }
  }
  TS(8);
  asm volatile("" ::"v"(pf.t));
  TSW(1);
}

#define QINV 24
#define TCH 2
constexpr float THR = 32.f;
struct K2C {
  rsrc_t rK, rV, rC;
  unsigned vo, ldsb;
  unsigned mS, mP;
  int n1, g, qrow;
  int rowA, rowB;
};
DI int k2_tile(const K2C& c, int n) { n = n > 16 ? 16 : n; return n < c.n1 ? n : 2 * n - c.n1 + c.g; }
DI unsigned k2_so(const K2C& c, int n) { return (n < c.n1 ? c.mS : (c.mP & 0xffffu)) + (unsigned)k2_tile(c, n) * 16384u; }
DI unsigned k2_m0(const K2C& c, int n) { return c.ldsb + (n < c.n1 ? c.mS : c.mP); }
DI LAS const char* k2_rd(const K2C& c, int n) { return (LAS const char*)(size_t)(c.vo + (c.ldsb + (n < c.n1 ? 0u : (c.mP & 0x10000u)))); }
template <int IMM> DI void dma16c(rsrc_t r, unsigned voff, unsigned soff, int a, int b) {
  asm volatile("s_cmp_lt_i32 %4, %5\n\ts_cbranch_scc1 .Ld%=\n\tbuffer_load_dwordx4 %0, %1, %2 offen offset:%3 lds\n.Ld%=:" ::"v"(voff), "s"(r), "s"(soff), "i"(IMM), "s"(a), "s"(b) : "scc", "memory");
}
DI void k2_mask(f32x16 (&s)[2], int dq) {
#pragma unroll
  for (int kb = 0; kb < 2; ++kb)
#pragma unroll
    for (int e = 0; e < 16; ++e) { const int cc = 32 * kb + (e & 3) + 8 * (e >> 2); if (cc > dq) s[kb][e] = -__builtin_inff(); }
}
DI float max3(float a, float b, float c) { float d; asm("v_max3_f32 %0, %1, %2, %3" : "=v"(d) : "v"(a), "v"(b), "v"(c)); return d; }
DI float k2_max(const f32x16 (&s)[2]) {
  float m0 = fmaxf(s[0][0], s[0][1]), m1 = fmaxf(s[1][0], s[1][1]);
#pragma unroll
  for (int e = 2; e < 16; e += 2) { m0 = fmaxf(fmaxf(m0, s[0][e]), s[0][e + 1]); m1 = fmaxf(fmaxf(m1, s[1][e]), s[1][e + 1]); }
  return fmaxf(m0, m1);
}
DI void addf(float& a, float b) { asm("v_add_f32 %0, %0, %1" : "+v"(a) : "v"(b)); }
DI void k2_loadq(u32x4 (&q)[8], unsigned la, int a, int b) {
  asm volatile("s_cmp_lg_u32 %9, %10\n\ts_cbranch_scc1 .Lq%=\n\t"
               "ds_read_b128 %0, %8\n\tds_read_b128 %1, %8 offset:1024\n\tds_read_b128 %2, %8 offset:2048\n\tds_read_b128 %3, %8 offset:3072\n\t"
               "ds_read_b128 %4, %8 offset:4096\n\tds_read_b128 %5, %8 offset:5120\n\tds_read_b128 %6, %8 offset:6144\n\tds_read_b128 %7, %8 offset:7168\n"
               ".Lq%=:" : "+v"(q[0]), "+v"(q[1]), "+v"(q[2]), "+v"(q[3]), "+v"(q[4]), "+v"(q[5]), "+v"(q[6]), "+v"(q[7])
               : "v"(la), "s"(a), "s"(b) : "scc", "memory");
}
struct K2S { f32x16 o[4], negm; u32x4 q[8]; bf16x8 f[3]; LAS const char* rk; float m, l; };

DI void k2_rescale(K2S& S, f32x16 (&sn)[2], float lmax, bool first) {
  float dl = swap32_max(lmax);
  if (first) { dl = dl == -__builtin_inff() ? 0.f : dl; S.m = dl; }
  else {
    dl = fmaxf(dl, 0.f); S.m += dl;
    const float f = __builtin_amdgcn_exp2f(-dl);
    S.l *= f;
#pragma unroll
    for (int db = 0; db < 4; ++db) S.o[db] *= f;
  }
#pragma unroll
  for (int kb = 0; kb < 2; ++kb) sn[kb] -= dl;
#pragma unroll
  for (int e = 0; e < 16; ++e) S.negm[e] = -S.m;
}
DI void k2_finishB(const K2C& c, K2S& S, unsigned ctx) {
  const float inv = __builtin_amdgcn_rcpf(swap32_add(S.l));
#pragma unroll
  for (int db = 0; db < 4; ++db) {
    const f32x16 res = S.o[db] * inv;
    bst16(pack8<0>(res), c.rC, c.vo, ctx + db * 2048); bst16(pack8<1>(res), c.rC, c.vo, ctx + db * 2048 + 1024);
    S.o[db] = (f32x16){};
  }
  S.l = 0.f;
}
DI void k2_finishA(const K2C& c, K2S& S, LAS char* lds, int wid, unsigned ctx) {
  const int lane = c.vo >> 4;
  const int g = wid >> 2, pw = wid ^ 4;
  LAS f32x4* ex = (LAS f32x4*)lds;
  LAS f32x2* es = (LAS f32x2*)(lds + 65536);
  const float lt = swap32_add(S.l);
  const float me = lt > 0.f ? S.m : -1e30f;
  es[wid * 64 + lane] = (f32x2){me, lt};
#pragma unroll
  for (int rd = 0; rd < 2; ++rd) {
    const f32x16 snd = g ? S.o[rd] : S.o[2 + rd];
#pragma unroll
    for (int e = 0; e < 4; ++e) ex[(wid * 8 + rd * 4 + e) * 64 + lane] = (f32x4){snd[4 * e], snd[4 * e + 1], snd[4 * e + 2], snd[4 * e + 3]};
  }
  WAITL0(); BAR();
  const f32x2 ps = es[pw * 64 + lane];
  const float mm = fmaxf(me, ps[0]);
  float w0 = __builtin_amdgcn_exp2f(me - mm), w1 = __builtin_amdgcn_exp2f(ps[0] - mm);
  const float inv = __builtin_amdgcn_rcpf(w0 * lt + w1 * ps[1]);
  w0 *= inv; w1 *= inv;
#pragma unroll
  for (int rd = 0; rd < 2; ++rd) {
    const f32x16 own = g ? S.o[2 + rd] : S.o[rd];
    f32x16 res;
#pragma unroll
    for (int e = 0; e < 4; ++e) {
      const f32x4 v = ex[(pw * 8 + rd * 4 + e) * 64 + lane];
#pragma unroll
      for (int c = 0; c < 4; ++c) res[4 * e + c] = own[4 * e + c] * w0 + v[c] * w1;
    }
    const unsigned p = ctx + (2 * g + rd) * 2048;
    bst16(pack8<0>(res), c.rC, c.vo, p); bst16(pack8<1>(res), c.rC, c.vo, p + 1024);
  }
}
template <int PAR> DI bf16x8 k2_frag(LAS const char* rd, int i) {
  if (i < 16) return ldsr(rd + (PAR ^ 1) * 16384 + i * 1024);
  if (i >= 32) return ldsr(rd + PAR * 16384 + (i - 32) * 1024);
  const int ks = (i - 16) >> 2, db = i & 3;
  return ldsr(rd + 32768 + PAR * 16384 + (ks >> 1) * 8192 + db * 2048 + (ks & 1) * 1024);
}
template <int PAR, bool NEXT> DI void k2_step(const K2C& c, K2S& S, int n, f32x16 (&sc)[2], f32x16 (&sn)[2], unsigned qnext, unsigned ctxB) {
  bf16x8 F[35];
  F[0] = S.f[0]; F[1] = S.f[1]; F[2] = S.f[2];
  u32x4 p[4];
  float l0 = S.l, l1 = 0.f, lmax = 0.f;
  unsigned soK = 0, soV = 0, soK2 = 0;
  LAS const char* rv = nullptr;
  if constexpr (NEXT) { soV = k2_so(c, n + 1); soK = k2_so(c, n + 3); soK2 = k2_so(c, n + 2); }
#pragma unroll
  for (int s = 0; s < 32; ++s) {
    if (s == 29) { asm volatile("s_waitcnt vmcnt(0) lgkmcnt(0)" ::: "memory"); BAR(); SB(); }
    if (s < 16) { if constexpr (NEXT) sn[s >> 3] = MFMA(F[s], __builtin_bit_cast(bf16x8, S.q[s & 7]), (s & 7) == 0 ? S.negm : sn[s >> 3]); }
    else S.o[s & 3] = MFMA(F[s], __builtin_bit_cast(bf16x8, p[(s - 16) >> 2]), S.o[s & 3]);
    SB();
    if (s == 0 && NEXT) F[3] = k2_frag<PAR>(S.rk, 3);
    if (s <= 27 && (NEXT || s >= 12)) F[s + 4] = k2_frag<PAR>(s < 12 ? S.rk : rv, s + 4);
    if (s >= 29 && NEXT) F[s + 3] = k2_frag<PAR>(S.rk, s + 3);
    if (s == 9) rv = k2_rd(c, n);
    if (s == 18 && NEXT) S.rk = k2_rd(c, n + 2);
    const int e0 = s < 5 ? 2 * s : s + 5, e1 = s < 5 ? 2 * s + 2 : s + 6;
    const int a0 = s < 6 ? 2 * s - 2 : s + 4, a1 = s < 6 ? 2 * s : s + 5;
    if (s <= 26) {
#pragma unroll
      for (int e = e0; e < e1; ++e) sc[e >> 4][e & 15] = __builtin_amdgcn_exp2f(sc[e >> 4][e & 15]);
    }
    if (s >= 1 && s <= 27) {
#pragma unroll
      for (int e = a0; e < a1; ++e) {
        if (e & 1) { addf(l1, sc[e >> 4][e & 15]); p[e >> 3][(e >> 1) & 3] = cvtpk(sc[e >> 4][(e & 15) - 1], sc[e >> 4][e & 15]); }
        else addf(l0, sc[e >> 4][e & 15]);
      }
    }
    if constexpr (NEXT) {
      if (s == 10) lmax = max3(sn[0][0], sn[0][1], sn[0][1]);
      if (s > 10 && s <= 17) lmax = max3(lmax, sn[0][2 * (s - 10)], sn[0][2 * (s - 10) + 1]);
      if (s >= 19 && s <= 26) lmax = max3(lmax, sn[1][2 * (s - 19)], sn[1][2 * (s - 19) + 1]);
      if (s == 1) dma16c<2048>(c.rK, c.vo, soK2, n + 2, c.n1);
      if (s == 3) dma16c<3072>(c.rK, c.vo, soK2, n + 2, c.n1);
      if (s == 5) dma16<0>(c.rV, c.vo, soV, k2_m0(c, n + 1) + 32768 + (PAR ^ 1) * 16384);
      if (s == 7) dma16n<1024>(c.rV, c.vo, soV);
      if (s == 9) dma16c<2048>(c.rV, c.vo, soV, n + 1, c.n1);
      if (s == 11) dma16c<3072>(c.rV, c.vo, soV, n + 1, c.n1);
      if (s == 16) k2_loadq(S.q, qnext, n + 2, c.g ? c.n1 : -1);
      if (s == 29) { dma16<0>(c.rK, c.vo, soK, k2_m0(c, n + 3) + (PAR ^ 1) * 16384); }
      if (s == 31) { dma16n<1024>(c.rK, c.vo, soK); }
    }
    if (s == 28) { S.l = l0 + l1; asm volatile("" : "+v"(S.l), "+v"(lmax)); }
    SB();
  }
  if constexpr (NEXT) {
    S.f[0] = F[32]; S.f[1] = F[33]; S.f[2] = F[34];
    const bool first = c.g && n + 1 == c.n1;
    const int kb = k2_tile(c, n + 1) * 64, rw = n + 1 < c.n1 ? c.rowB : c.rowA;
    if (kb + 63 > rw) { k2_mask(sn, rw + c.qrow - kb); lmax = k2_max(sn); }
    if (first || __any(lmax > THR)) k2_rescale(S, sn, lmax, first);
    if (first) k2_finishB(c, S, ctxB);
    if (c.g && n + 2 == c.n1) {
#pragma unroll
      for (int e = 0; e < 16; ++e) S.negm[e] = 0.f;
    }
    SB();
  }
}
DI void ld16(u32x4& v, rsrc_t r, unsigned vo, unsigned so) { asm volatile("buffer_load_dwordx4 %0, %1, %2, %3 offen sc1" : "=v"(v) : "v"(vo), "s"(r), "s"(so) : "memory"); }
struct QX {
  const FL& fl; const K2C& c; rsrc_t rz; unsigned voF, voZ, zo[2]; LAS char* kw; int bid, wid; u32x2 fk; u32x4 zv[2], kv[6];
  static constexpr int extra(int t) { return t == 0 ? 4 : t == 2 ? 6 : t == 3 ? 1 : t == 6 ? 2 : 0; }
  template <int N> DI void at() {
    if constexpr (N == 0) fl_ld(fl, fk, voF);
    if constexpr (N == 7) fl_pub(fl, F_Z + bid * 8, wid == 0 && c.vo == 0);
    if constexpr (N == 12) fl_ld(fl, fk, voZ);
    if constexpr (N == 19) { if (wid == 0) fl_chk<false>(fl, fk, voZ); }
    if constexpr (N == 23) { ld16(zv[0], rz, zo[0], 0); ld16(zv[1], rz, zo[1], 0); }
    if constexpr (N == QINV) { if (wid == 0) asm volatile("buffer_inv sc1" ::: "memory"); }
    if constexpr (N >= 8 && N < 11) { ld16(kv[2 * N - 16], N == 10 ? c.rV : c.rK, c.vo, c.mS + (N == 9) * 16384); ld16(kv[2 * N - 15], N == 10 ? c.rV : c.rK, c.vo, c.mS + (N == 9) * 16384 + 1024); }
    if constexpr (N >= 16 && N < 19) {
      asm volatile("" : "+v"(kv[2 * N - 32]), "+v"(kv[2 * N - 31]));
      *(LAS u32x4*)(kw + (N - 16) * 16384) = kv[2 * N - 32]; *(LAS u32x4*)(kw + (N - 16) * 16384 + 1024) = kv[2 * N - 31];
    }
  }
  template <int T> DI void pre() { if constexpr (T == 1) { if (wid == 0) { asm volatile("s_waitcnt vmcnt(9)" : "+v"(fk)::"memory"); fl_chk<false>(fl, fk, voF); } } }
};
template <int N> DI void ln_part(const u32x4 (&v)[N], LAS f32x2* st, int wid, int lane) {
  float s = 0.f, q = 0.f;
#pragma unroll
  for (int i = 0; i < N; ++i)
#pragma unroll
    for (int e = 0; e < 4; ++e) { const float a = bflo(v[i][e]), b = bfhi(v[i][e]); s += a + b; q = fmaf(a, a, fmaf(b, b, q)); }
  s += __shfl_xor(s, 8); q += __shfl_xor(q, 8); s += __shfl_xor(s, 16); q += __shfl_xor(q, 16);
  s = swap32_add(s); q = swap32_add(q);
  if (lane < 8) st[lane * 8 + wid] = (f32x2){s, q};
}
template <int N> DI void ln_apply(u32x4 (&v)[N], LAS const f32x2* st, int lane, float invn) {
  float s = 0.f, q = 0.f;
#pragma unroll
  for (int i = 0; i < 4; ++i) { const f32x4 t = *(LAS const f32x4*)(st + (lane & 7) * 8 + 2 * i); s += t[0] + t[2]; q += t[1] + t[3]; }
  float mb, rs; ln_stats(s, q, invn, mb, rs);
#pragma unroll
  for (int i = 0; i < N; ++i)
#pragma unroll
    for (int e = 0; e < 4; ++e) v[i][e] = cvtpk((bflo(v[i][e]) - mb) * rs, (bfhi(v[i][e]) - mb) * rs);
}

constexpr int k3_cum(int t) { const int a = 2 * t + 4, b = t + 6; return a < b ? a : b < 32 ? b : 32; }
struct WO {
  rsrc_t rw; unsigned vo, vl, l0, so;
  DI void dma(int pc) const { dma16<0>(rw, pc >= 9 ? vl : vo, so + pc * 8192, l0 + pc * 1024); }
  static constexpr int extra(int t) { return t >= 2 && t < 13; }
  template <int N> DI void at() { if constexpr (N % 4 == 1 && N / 4 >= 2 && N / 4 < 13) dma(N / 4 - 2); }
  template <int T> DI void pre() {}
};
struct K3B { rsrc_t rA, rB; unsigned voA[2], voB[2], soA, m0; LAS const char* aR; LAS const char* bR; };
struct K3F { bf16x8 a[2], w[3]; };
template <int T, int P> DI void k3b_dma(const K3B& c) {
  if constexpr (T < 32) {
    if constexpr (P == 0) dma16<0>(c.rA, c.voA[0], c.soA + T * 128, c.m0 + ((T + 3) % 5) * 32768);
    if constexpr (P == 1) dma16n<1024>(c.rA, c.voA[1], c.soA + T * 128);
    if constexpr (P == 2) dma16<0>(c.rB, c.voB[0], T * (64 * ZLD * 2), c.m0 + ((T + 3) % 5) * 32768 + 16384);
    if constexpr (P == 3) dma16n<1024>(c.rB, c.voB[1], T * (64 * ZLD * 2));
  }
}
template <int T, int G> DI void k3b_rd(const K3B& c, K3F& f) {
  if constexpr (T < 32) {
    constexpr int o = ((T + 3) % 5) * 32768;
    if constexpr (G == 1) { f.a[0] = ldsr(c.aR + o); f.a[1] = ldsr(c.aR + o + 4096); }
    else { constexpr int j = G ? G - 1 : 0; f.w[j] = ldstr(c.bR + o + j * 64, c.bR + o + j * 64 + 8 * 192); }
  }
}
template <int T> DI void k3b_stage(const K3B& c, const K3F& f, K3F& fn, f32x16 (&o)[6]) {
  constexpr int N = k3_cum(T - 1), M = k3_cum(T) - N == 2 ? N + 1 : 32;
  if constexpr (T < 31) { WAITVL(4 * (N - T - 2) + (T == 0 ? 4 : 0)); BAR(); SB(); }
#define BM(i, j) o[3 * (i) + (j)] = MFMA(f.w[j], f.a[i], o[3 * (i) + (j)]); SB()
  BM(0, 0); k3b_rd<T + 1, 0>(c, fn); SB(); BM(1, 0); k3b_rd<T + 1, 1>(c, fn); k3b_dma<N, 0>(c); SB(); BM(0, 1); k3b_rd<T + 1, 2>(c, fn); k3b_dma<N, 1>(c); SB();
  BM(1, 1); k3b_rd<T + 1, 3>(c, fn); k3b_dma<N, 2>(c); SB(); BM(0, 2); k3b_dma<N, 3>(c); k3b_dma<M, 0>(c); k3b_dma<M, 1>(c); SB(); BM(1, 2); k3b_dma<M, 2>(c); k3b_dma<M, 3>(c); SB();
}
template <int T> DI void k3b_run(const K3B& c, K3F& f0, K3F& f1, f32x16 (&o)[6]) {
  if constexpr (T < 32) { k3b_stage<T>(c, f0, f1, o); k3b_stage<T + 1>(c, f1, f0, o); k3b_run<T + 2>(c, f0, f1, o); }
}
template <int S> DI void unit_put(LAS char* p, const f32x16& a) {
  *(LAS f32x4*)p = (f32x4){a[8 * S], a[8 * S + 1], a[8 * S + 2], a[8 * S + 3]}; *(LAS f32x4*)(p + 1024) = (f32x4){a[8 * S + 4], a[8 * S + 5], a[8 * S + 6], a[8 * S + 7]};
}
template <int KG, int J> DI void unit_send(LAS char* ex, const f32x16 (&o)[2 * J], float (&v)[J][8]) {
#pragma unroll
  for (int u = 0; u < 4; ++u)
#pragma unroll
    for (int j = 0; j < J; ++j) {
      if (u == KG) {
#pragma unroll
        for (int e = 0; e < 8; ++e) { v[j][e] = o[J * (u >> 1) + j][8 * (u & 1) + e]; asm volatile("" : "+v"(v[j][e])); }
      } else {
        LAS char* p = ex + ((u * 3 + (KG > u ? KG - 1 : KG)) * J + j) * 2048;
        if (u & 1) unit_put<1>(p, o[J * (u >> 1) + j]); else unit_put<0>(p, o[J * (u >> 1) + j]);
      }
    }
}
template <int J> DI void unit_reduce(LAS char* ex, int kg, const f32x16 (&o)[2 * J], float (&v)[J][8]) {
  if (kg == 0) unit_send<0, J>(ex, o, v); else if (kg == 1) unit_send<1, J>(ex, o, v); else if (kg == 2) unit_send<2, J>(ex, o, v); else unit_send<3, J>(ex, o, v);
  WAITL0(); BAR(); SB();
  ex += kg * (3 * J * 2048);
#pragma unroll
  for (int j = 0; j < J; ++j)
#pragma unroll
    for (int s = 0; s < 3; ++s) {
      const f32x4 a = *(LAS const f32x4*)(ex + (s * J + j) * 2048), b = *(LAS const f32x4*)(ex + (s * J + j) * 2048 + 1024);
#pragma unroll
      for (int e = 0; e < 4; ++e) { v[j][e] += a[e]; v[j][4 + e] += b[e]; }
    }
}

__global__ __launch_bounds__(512, 2) void mega(Args) {
  extern __shared__ __attribute__((aligned(16))) char lds_[];
  LAS char* lds = (LAS char*)lds_;
  const unsigned ldsb = (unsigned)(size_t)lds;
  FL fl;
  GC q; GR qr;
  p1_kvz(lds, fl, q, qr);

  int tid = threadIdx.x;
  asm volatile("" : "+v"(tid));
  char* ws = kargs()->ws;
  const int wid = rfl(tid >> 6);
  const int bid = blockIdx.x, xcd = bid & 7, idx = bid >> 3;
  TSD; TS(0);
  K2C c;
  K2S S;
  unsigned ctxA;
  {
    const int lane = tid & 63, r = lane & 31, h = lane >> 5, q4 = (lane >> 2) & 3;
    const int head = xcd * 4 + (idx >> 3), j = idx & 7, g = wid >> 2, wq = wid & 3;
    c.rK = mkrsrc(ws + WS_K + (size_t)head * 524288, 524288); c.rV = mkrsrc(ws + WS_V + (size_t)head * 524288, 524288);
    c.rC = mkrsrc(ws + WS_C, 16u << 20);
    c.vo = lane * 16; c.ldsb = ldsb; c.mS = wid * 2048; c.mP = g * 65536 + wq * 4096;
    c.n1 = 2 * j + 2; c.g = g; c.qrow = r - 4 * h;
    const int rbA = (15 - j) * 4 + wq, rbB = g ? j * 4 + wq : rbA;
    c.rowA = rbA * 32; c.rowB = rbB * 32;
    ctxA = (rbA * 32 + head) * 8192;
    const unsigned ctxB = (rbB * 32 + head) * 8192;
    const int ch = tid >> 3, zr = bid * 8 + (lane & 7), zcg = 7 + (lane & 7);
    QX x{fl, c};
    x.bid = bid; x.wid = wid; x.rz = mkrsrc(ws + WS_Z2, SEQ * FH * 2);
    x.voF = F_KV + (((idx & ~7) + (lane & 7)) * 8 + xcd) * 8; x.voZ = F_Z + ((((bid >> 4) & 3) * 8 + (zcg & 7)) * 8 + (bid >> 6) * 2 + (zcg >> 3)) * 8;
    x.kw = lds + c.mS + lane * 16;
    x.zo[0] = (zr >> 5) * 43 * 1024 + (ch >> 1) * 1024 + (ch & 1) * 512 + (zr & 31) * 16; x.zo[1] = ch < 22 ? x.zo[0] + 32768 : 0x80000000u;
    LAS f32x2* st = (LAS f32x2*)(lds + 98304);
#pragma unroll
    for (int i = 0; i < 4; ++i) { q.bR[i] = lds + 65536 + (8 * h + q4) * 256 + ((i ^ q4) * 64) + ((lane >> 4) & 1) * 32 + (lane & 3) * 8; S.o[i] = (f32x16){}; }
    TS(1);
    WAITV(7); BAR(); SB();
    TS(2);
    gc_first<8, 1024>(q, qr);
    gc_run<8, 8192, 1024, 0>(q, qr, S.o, x);
    TS(3);
#pragma unroll
    for (int i = 0; i < 4; ++i) {
      S.o[i] *= QSCALE;
      S.q[2 * i] = pack8<0>(S.o[i]); S.q[2 * i + 1] = pack8<1>(S.o[i]);
      S.o[i] = (f32x16){};
    }
    if (!g) {
#pragma unroll
      for (int i = 0; i < 8; ++i) *(LAS u32x4*)(lds + 131072 + wq * 8192 + i * 1024 + lane * 16) = S.q[i];
    }
    S.negm = (f32x16){}; S.m = 0.f; S.l = 0.f;
    f32x16 sa[2], sb[2];
    const unsigned qA = c.ldsb + 131072 + wq * 8192 + c.vo;
    S.rk = lds + lane * 16;
#pragma unroll
    for (int s = 0; s < 16; ++s) sa[s >> 3] = MFMA(k2_frag<1>(S.rk, s), __builtin_bit_cast(bf16x8, S.q[s & 7]), (s & 7) == 0 ? S.negm : sa[s >> 3]);
    asm volatile("s_waitcnt vmcnt(0)" : "+v"(x.zv[0]), "+v"(x.zv[1])::"memory");
    ln_part<2>(x.zv, st, wid, lane);
    if (63 > c.rowB) k2_mask(sa, c.rowB + c.qrow);
    k2_rescale(S, sa, k2_max(sa), true);
    SB();
    WAITL0(); BAR(); SB();
    dma16<0>(c.rK, c.vo, k2_so(c, 2), k2_m0(c, 2)); dma16n<1024>(c.rK, c.vo, k2_so(c, 2));
#pragma unroll
    for (int i = 0; i < 3; ++i) S.f[i] = k2_frag<0>(S.rk, i);
    {
      ln_apply<2>(x.zv, st, lane, 1.f / FH);
      const rsrc_t ro = mkrsrc(ws + WS_Z2N, SEQ * ZLD * 2);
      const unsigned oo = ((zr & ~12) | ((zr & 4) << 1) | ((zr & 8) >> 1)) * (ZLD * 2) + ch * 16;
      if (ch >= 22) x.zv[1] = ((u32x4){0u, 0u, 0u, 0u});
      bst16(x.zv[0], ro, oo, 0); bst16(x.zv[1], ro, ch < 32 ? oo + 1024 : 0x80000000u, 0);
    }
    unsigned pf = 0;
    TS(4);
    for (int n = 0; n < 16; n += 2) {
      k2_step<0, true>(c, S, n, sa, sb, qA, ctxB);
      if (n == 0) fl_pub(fl, F_ZN + bid * 8, wid == 0 && c.vo == 0);
      if (n == 8 && xcd >= 4 && wid == 0) {
        u32x2 fz[4];
#pragma unroll
        for (int i = 0; i < 4; ++i) fl_ld(fl, fz[i], F_ZN + i * 512 + (c.vo >> 1));
        WAITV(0);
        fl_chk<false>(fl, fz[0], F_ZN + (c.vo >> 1)); fl_chk<false>(fl, fz[1], F_ZN + 512 + (c.vo >> 1));
        fl_chk<false>(fl, fz[2], F_ZN + 1024 + (c.vo >> 1)); fl_chk<true>(fl, fz[3], F_ZN + 1536 + (c.vo >> 1));
        SB();
      }
      if (n >= TCH && n < TCH + 6) {
        const KA ka = kargs();
        const unsigned w = bid * 8 + wid;
        const rsrc_t rp = n == TCH ? mkrsrc(ka->wsp, SEQ * SEQ * 2) : n == TCH + 2 ? mkrsrc(ka->wo, 4096 * DM * 2) : mkrsrc(ka->wout, FH * DM * 2);
        asm volatile("buffer_load_dword %0, %1, %2, %3 offen" : "+v"(pf) : "v"(c.vo * 4), "s"(rp), "s"(n == TCH + 2 ? (w & 1023) * 4096 : w < 2048 - (n - TCH) * 469 ? w * 4096 : 0) : "memory");
        SB();
      }
      k2_step<1, true>(c, S, n + 1, sb, sa, qA, ctxB);
    }
    asm volatile("" ::"v"(pf));
    TS(5);
    k2_step<0, false>(c, S, 16, sa, sb, qA, ctxB);
    TS(6);
  }
  GC ca; K3B cb;
  asm volatile("" : "+v"(c.vo));
  const int lane = c.vo >> 4, r = lane & 31, h = lane >> 5, q4 = (lane >> 2) & 3;
  const int rb3 = xcd < 4 ? idx >> 2 : (xcd - 4) * 4 + (idx >> 3), wm = wid & 1, kg = wid >> 1;
  const int k16 = 6 * (idx & 7) + (kg & 1);
  const unsigned zo = ((rb3 * 4 + 2 * wm + (kg >> 1)) * 43 + k16) * 1024;
  if (xcd < 4) {
    ca.rA = c.rC; ca.rB = mkrsrc(kargs()->wo, 4096 * DM * 2);
    ca.vo = lane * 16; ca.voB[0] = (lane >> 4) * 1024 + (((lane & 15) ^ ((lane >> 4) << 2)) * 16); ca.voB[1] = ca.voB[0] + 3072;
    ca.soA = ((rb3 * 8 + wid) * 256 + xcd * 64) * 1024; ca.soB = (xcd * 1024 + wid * 8) * 1024 + (idx & 3) * 256; ca.m0 = ldsb + 98304 + wid * 2048;
    gc_dma4<16, 1024>(ca);
  } else {
    const KA ka = kargs();
    cb.rA = mkrsrc(ka->wsp, SEQ * SEQ * 2); cb.rB = mkrsrc(ws + WS_Z2N, SEQ * ZLD * 2);
#pragma unroll
    for (int i = 0; i < 2; ++i) {
      cb.voA[i] = (8 * i + (lane >> 3)) * 4096 + (((lane & 7) ^ (4 * i + (lane >> 4))) * 16) - i * 1024;
      const int o = (2 * wid + i) * 64 + lane;
      cb.voB[i] = wid < 6 ? (o / 12) * (ZLD * 2) + (o % 12) * 16 + (idx & 7) * 192 - i * 1024 : 0x80000000u;
    }
    cb.soA = (rb3 * 128 + wid * 16) * 4096; cb.m0 = ldsb + wid * 2048;
#define B4(T) k3b_dma<T, 0>(cb); k3b_dma<T, 1>(cb); k3b_dma<T, 2>(cb); k3b_dma<T, 3>(cb)
    B4(0); B4(1);
  }
  SB();
  k2_finishA(c, S, lds, wid, ctxA);
  TS(7); WAITV(0); BAR(); SB();
  fl_pub(fl, F_CTX + bid * 128, wid == 0 && lane == 0);
  TS(8); TSW(2); TSR; TS(0);
  WO wo;
  wo.rw = mkrsrc(kargs()->wout, FH * DM * 2); wo.vo = (lane >> 3) * 1024 + (((lane & 7) ^ (((lane >> 4) & 1) << 2)) * 16); wo.vl = wid == 7 ? 0x80000000u : wo.vo;
  wo.l0 = ldsb + wid * 11264; wo.so = wid * 11 * 8192 + (idx >> 2) * 128;
  if (xcd < 4) {
    GR g;
    if (wid == 0) {
      const int hd = xcd * 8 + (lane & 7), blk = 2 * rb3 + ((lane >> 3) & 1);
      fl_chk<false>(fl, (u32x2){~fl.lo, 0u}, F_CTX + (((hd & 3) * 8 + (blk < 8 ? blk : 15 - blk)) * 8 + (hd >> 2)) * 128);
    }
    BAR(); SB();
    TS(1);
    gc_lda2<16>(ca, g);
#pragma unroll
    for (int i = 0; i < 4; ++i) ca.bR[i] = lds + 98304 + (4 * h + q4) * 256 + ((i ^ q4) * 64) + ((lane >> 4) & 1) * 32 + (lane & 3) * 8;
    f32x16 o[4] = {};
    WAITV(4); SB();
    TS(2);
    gc_first<16, 2048>(ca, g);
    gc_run<16, 1024, 2048, 0>(ca, g, o, wo);
    TS(3);
    const rsrc_t rp = mkrsrc(ws + WS_P, 8u << 20);
    const unsigned so = ((xcd * 64 + rb3 * 8 + wid) * 16 + (idx & 3) * 4) * 2048;
#pragma unroll
    for (int i = 0; i < 4; ++i) { bst16(pack8<0>(o[i]), rp, ca.vo, so + i * 2048); bst16(pack8<1>(o[i]), rp, ca.vo, so + i * 2048 + 1024); }
  } else {
    u32x4 z[3]; float b;
    const rsrc_t rz = mkrsrc(ws + WS_Z1, SEQ * FH * 2);
#pragma unroll
    for (int j = 0; j < 3; ++j) asm volatile("buffer_load_dwordx4 %0, %1, %2, %3 offen sc1" : "=v"(z[j]) : "v"(lane * 16), "s"(rz), "s"(zo + j * 2048) : "memory");
    asm volatile("buffer_load_ushort %0, %1, %2, %3 offen" : "=v"(b) : "v"(r * 2), "s"(mkrsrc(kargs()->bsp, SEQ * 2)), "s"((rb3 * 4 + 2 * wm + (kg >> 1)) * 64) : "memory");
    cb.aR = lds + (wm * 64 + r) * 128 + ((h ^ ((r >> 1) & 7) ^ (2 * kg)) * 16);
    cb.bR = lds + 16384 + (16 * kg + 4 * h + q4) * 192 + ((lane >> 4) & 1) * 32 + (lane & 3) * 8;
    f32x16 o[6] = {};
    K3F f0, f1;
    k3b_rd<0, 0>(cb, f0); k3b_rd<0, 1>(cb, f0); k3b_rd<0, 2>(cb, f0); k3b_rd<0, 3>(cb, f0); SB();
    k3b_run<0>(cb, f0, f1, o);
    TS(3);
    BAR();
    asm volatile("" : "+v"(z[0]), "+v"(z[1]), "+v"(z[2]), "+v"(b));
    b = __uint_as_float(__float_as_uint(b) << 16);
    float v[3][8];
    unit_reduce<3>(lds + wm * 73728 + lane * 16, kg, o, v);
    const rsrc_t rh = mkrsrc(ws + WS_H, SEQ * FH * 2);
#pragma unroll
    for (int j = 0; j < 3; ++j) {
      u32x4 w;
#pragma unroll
      for (int e = 0; e < 4; ++e) w[e] = cvtpk(bflo(z[j][e]) * (v[j][2 * e] + b), bfhi(z[j][e]) * (v[j][2 * e + 1] + b));
      if (k16 + 2 * j < 43) bst16(w, rh, lane * 16, zo + j * 2048);
    }
    fl_pub(fl, 0, bid == 7 && wid == 0 && lane == 0);
  }
  {
    const int rb = xcd * 4 + (idx & 3), cb = idx >> 2, rb32 = rb * 2 + wm;
    u32x4 hf[11], pp[4]; u32x2 xv[2], fh;
    const unsigned voH = F_P + ((((rb >> 1) & 3) * 8 + (lane & 7)) * 8 + 4 + (rb >> 3)) * 128, voP = F_P + ((xcd * 4 + (cb >> 1)) * 8 + (lane & 3)) * 128;
    fl_ld(fl, fh, voH);
    TS(4); WAITV(0); BAR(); SB();
    fl_pub(fl, F_P + bid * 128, wid == 0 && lane == 0);
    TS(5); TSW(3); TSR; TS(0);
    const KA ka = kargs();
    if (xcd >= 4) {
#pragma unroll
      for (int pc = 0; pc < 11; ++pc) wo.dma(pc);
    }
    if (wid == 7) fl_chk<false>(fl, fh, voH);
    BAR(); SB();
    TS(1);
    {
      const rsrc_t rh = mkrsrc(ws + WS_H, SEQ * FH * 2), rx = mkrsrc(ka->x, SEQ * DM * 2);
#pragma unroll
      for (int i = 0; i < 11; ++i) ld16(hf[i], rh, i == 10 && kg == 3 ? 0x80000000u : lane * 16, (rb32 * 43 + kg * 11 + i) * 1024);
      fl_ld(fl, fh, voP);
#pragma unroll
      for (int i = 0; i < 2; ++i)
        asm volatile("buffer_load_dwordx2 %0, %1, %2, %3 offen offset:%4" : "=v"(xv[i]) : "v"(r * 1024 + h * 8), "s"(rx), "s"(rb32 * 32768 + cb * 128 + kg * 32), "i"(i * 16) : "memory");
    }
    LAS const char* bR[2];
#pragma unroll
    for (int j = 0; j < 2; ++j) bR[j] = lds + kg * 22528 + (4 * h + q4) * 128 + ((j ^ (q4 >> 1)) * 64) + ((lane >> 4) & 1) * 32 + (lane & 3) * 8;
    f32x16 o[2] = {};
    WAITV(3); BAR(); SB();
    TS(2);
#pragma unroll
    for (int i = 0; i < 11; ++i) asm volatile("" : "+v"(hf[i]));
#pragma unroll
    for (int ks = 0; ks < 11; ++ks)
#pragma unroll
      for (int j = 0; j < 2; ++j) o[j] = MFMA(ldstr(bR[j] + ks * 2048, bR[j] + ks * 2048 + 1024), __builtin_bit_cast(bf16x8, hf[ks]), o[j]);
    TS(3);
    WAITVL(0);
    if (wid == 7) fl_chk<false>(fl, fh, voP);
    BAR(); SB();
    const rsrc_t rp = mkrsrc(ws + WS_P, 8u << 20);
#pragma unroll
    for (int i = 0; i < 4; ++i) ld16(pp[i], rp, lane * 16, ((i * 64 + rb32) * 32 + cb * 4 + kg) * 1024);
    float v[1][8];
    unit_reduce<1>(lds + wm * 24576 + lane * 16, kg, o, v);
    asm volatile("s_waitcnt vmcnt(0)" : "+v"(pp[0]), "+v"(pp[1]), "+v"(pp[2]), "+v"(pp[3]), "+v"(xv[0]), "+v"(xv[1])::"memory");
    u32x4 w;
#pragma unroll
    for (int e = 0; e < 4; ++e)
      w[e] = cvtpk(v[0][2 * e] + bflo(xv[e >> 1][e & 1]) + ((bflo(pp[0][e]) + bflo(pp[1][e])) + (bflo(pp[2][e]) + bflo(pp[3][e]))),
                   v[0][2 * e + 1] + bfhi(xv[e >> 1][e & 1]) + ((bfhi(pp[0][e]) + bfhi(pp[1][e])) + (bfhi(pp[2][e]) + bfhi(pp[3][e]))));
    bst16<0>(natural8(w), mkrsrc(ka->out, SEQ * DM * 2), r * 1024 + h * 16, rb32 * 32768 + cb * 128 + kg * 32);
    TS(4); TSW(4);
  }
}

extern "C" void kernel_launch(void* const* d_in, const int* in_sizes, int n_in, void* d_out, int out_size, void* d_ws, size_t ws_size, hipStream_t stream) {
  (void)in_sizes; (void)n_in; (void)out_size; (void)ws_size;
  const Args a = {(const u16*)d_in[0], (const u16*)d_in[1], (const u16*)d_in[2], (const u16*)d_in[3], (const u16*)d_in[4], (const u16*)d_in[5],
                  (const u16*)d_in[7], (const u16*)d_in[8], (const u16*)d_in[9], (const u16*)d_in[10], (char*)d_ws, (u16*)d_out};
  (void)hipFuncSetAttribute((const void*)mega, hipFuncAttributeMaxDynamicSharedMemorySize, LDSZ);
  mega<<<256, 512, LDSZ, stream>>>(a);
}
```

```cpp
#include <hip/hip_runtime.h>
#include <hip/hip_bf16.h>
#include <stdint.h>

typedef __bf16 bf16x8 __attribute__((ext_vector_type(8)));
typedef __bf16 bf16x4 __attribute__((ext_vector_type(4)));
typedef __bf16 bf16x2 __attribute__((ext_vector_type(2)));
typedef float f32x16 __attribute__((ext_vector_type(16)));
typedef float f32x4 __attribute__((ext_vector_type(4)));
typedef float f32x2 __attribute__((ext_vector_type(2)));
typedef unsigned u32x4 __attribute__((ext_vector_type(4)));
typedef unsigned u32x2 __attribute__((ext_vector_type(2)));
typedef unsigned short u16;
typedef __amdgpu_buffer_rsrc_t rsrc_t;
#define LAS __attribute__((address_space(3)))
#define DI __device__ __forceinline__
#define SB() __builtin_amdgcn_sched_barrier(0)
#define MFMA(a, b, c) __builtin_amdgcn_mfma_f32_32x32x16_bf16((a), (b), (c), 0, 0, 0)
#define WAITV(n) asm volatile("s_waitcnt vmcnt(" #n ")" ::: "memory")
#define WAITL0() asm volatile("s_waitcnt lgkmcnt(0)" ::: "memory")
#define BAR() do { asm volatile("" ::: "memory"); __builtin_amdgcn_s_barrier(); asm volatile("" ::: "memory"); } while (0)

#define TSD
#define TSR
#define TS(i)
#define TSW(k)
constexpr int SEQ = 2048, DM = 512, NH = 32, HD = 128, FH = 688, FI = 1376, ZLD = 768;
constexpr size_t WS_XN = 0;
constexpr size_t WS_Z1 = WS_XN + (size_t)SEQ * DM * 2;
constexpr size_t WS_Z2 = WS_Z1 + (size_t)SEQ * FH * 2;
constexpr size_t WS_Z2N = WS_Z2 + (size_t)SEQ * FH * 2;
constexpr size_t WS_H = WS_Z2N + (size_t)SEQ * ZLD * 2;
constexpr size_t WS_K = 48u << 20;
constexpr size_t WS_V = WS_K + (16u << 20);
constexpr size_t WS_C = WS_V + (16u << 20);
constexpr size_t WS_P = WS_C + (16u << 20);
constexpr size_t WS_F = 120u << 20;
static_assert(WS_H + (size_t)SEQ * FH * 2 <= WS_K, "ws map");

DI unsigned rfl(unsigned v) { return __builtin_amdgcn_readfirstlane(v); }
DI unsigned cvtpk(float lo, float hi) { f32x2 v = {lo, hi}; bf16x2 b = __builtin_convertvector(v, bf16x2); return __builtin_bit_cast(unsigned, b); }
DI float bflo(unsigned u) { return __uint_as_float(u << 16); }
DI float bfhi(unsigned u) { return __uint_as_float(u & 0xffff0000u); }
DI float rbf(float v) { return bflo(cvtpk(v, v) & 0xffffu); }
template <int S> DI u32x4 pack8(const f32x16& c) {
  u32x4 w = {cvtpk(c[8 * S + 0], c[8 * S + 1]), cvtpk(c[8 * S + 2], c[8 * S + 3]), cvtpk(c[8 * S + 4], c[8 * S + 5]), cvtpk(c[8 * S + 6], c[8 * S + 7])};
  return w;
}
DI rsrc_t mkrsrc(const void* p, unsigned bytes) { return __builtin_amdgcn_make_buffer_rsrc((void*)p, (short)0, (int)bytes, 0x00020000); }
template <int IMM> DI void dma16(rsrc_t r, unsigned voff, unsigned soff, unsigned m0v) {
  asm volatile("s_mov_b32 m0, %0\n\ts_nop 0\n\tbuffer_load_dwordx4 %1, %2, %3 offen offset:%4 lds" ::"s"(m0v), "v"(voff), "s"(r), "s"(soff), "i"(IMM) : "memory");
}
DI bf16x8 ldsr(LAS const char* p) { return *(LAS const bf16x8*)p; }
DI bf16x8 ldstr(LAS const char* p0, LAS const char* p1) {
  bf16x4 a = __builtin_amdgcn_ds_read_tr16_b64_v4bf16((LAS bf16x4*)p0);
  bf16x4 b = __builtin_amdgcn_ds_read_tr16_b64_v4bf16((LAS bf16x4*)p1);
  return __builtin_shufflevector(a, b, 0, 1, 2, 3, 4, 5, 6, 7);
}
DI float swap32_add(float v) { auto r = __builtin_amdgcn_permlane32_swap(__float_as_uint(v), __float_as_uint(v), false, false); return __uint_as_float(r[0]) + __uint_as_float(r[1]); }
DI float swap32_max(float v) { auto r = __builtin_amdgcn_permlane32_swap(__float_as_uint(v), __float_as_uint(v), false, false); return fmaxf(__uint_as_float(r[0]), __uint_as_float(r[1])); }
DI u32x4 natural8(u32x4 w) {
  const auto a = __builtin_amdgcn_permlane32_swap(w[0], w[2], false, false), b = __builtin_amdgcn_permlane32_swap(w[1], w[3], false, false);
  return (u32x4){a[0], b[0], a[1], b[1]};
}
template <int IMM> DI void dma16n(rsrc_t r, unsigned voff, unsigned soff) {
  asm volatile("buffer_load_dwordx4 %0, %1, %2 offen offset:%3 lds" ::"v"(voff), "s"(r), "s"(soff), "i"(IMM) : "memory");
}
template <int AUX = 16> DI void bst16(u32x4 v, rsrc_t r, unsigned vo, unsigned so) { __builtin_amdgcn_raw_buffer_store_b128(v, r, vo, so, AUX); }
#define WAITVL(n) asm volatile("s_waitcnt vmcnt(%0) lgkmcnt(0)" ::"i"(n) : "memory")
DI void ln_stats(float s, float q, float invn, float& mb, float& rs) {
  const float mean = s * invn, var = fmaxf(fmaf(-mean, mean, q * invn), 0.f);
  mb = rbf(mean); rs = 1.f / rbf(sqrtf(rbf(rbf(var) + 1e-5f)));
}
DI float dot2(unsigned a, unsigned b, float c) { return __builtin_amdgcn_fdot2_f32_bf16(__builtin_bit_cast(bf16x2, a), __builtin_bit_cast(bf16x2, b), c, false); }
enum { F_XN = 4096, F_KV = 8192, F_ZN = 12288, F_Z = 16384, F_CTX = 65536, F_P = 131072 };
struct FL { rsrc_t r; unsigned lo, hi; };
struct Args { const u16 *x, *ckv, *wq, *wk, *wv, *wo, *w_in, *wsp, *bsp, *wout; char* ws; u16* out; };
typedef __attribute__((address_space(4))) const Args* KA;
DI KA kargs() { KA a = (KA)__builtin_amdgcn_kernarg_segment_ptr(); asm volatile("" : "+s"(a)); return a; }
DI void fl_pub(const FL& f, unsigned off, bool lead) { if (lead) __builtin_amdgcn_raw_buffer_store_b64((u32x2){f.lo, f.hi}, f.r, 0, off, 16); }
DI void fl_ld(const FL& f, u32x2& v, unsigned vo) { asm volatile("buffer_load_dwordx2 %0, %1, %2, 0 offen sc1" : "=v"(v) : "v"(vo), "s"(f.r) : "memory"); }
template <bool ACQ> DI void fl_chk(const FL& f, u32x2 v, unsigned vo) {
  asm volatile("" : "+v"(v));
#pragma unroll 1
  for (int i = 0; i < (1 << 18) && __any(v[0] != f.lo || v[1] != f.hi); ++i) {
    __builtin_amdgcn_s_sleep(2); fl_ld(f, v, vo);
    asm volatile("s_waitcnt vmcnt(0)" : "+v"(v)::"memory");
  }
  if (ACQ) asm volatile("buffer_inv sc1" ::: "memory");
}
struct GC { rsrc_t rA, rB; unsigned vo, voB[2], soA, soB, m0; LAS const char* bR[4]; };
struct GR { u32x4 a[3][4]; bf16x8 w[2][4]; };
template <int NP, int LDB, int T, int P> DI void gc_dma(const GC& c) {
  if constexpr (T < NP) { if constexpr (P == 0) dma16<0>(c.rB, c.voB[0], c.soB + T * 64 * LDB, c.m0 + (T & 3) * 16384); else dma16n<1024>(c.rB, c.voB[1], c.soB + T * 64 * LDB); }
}
template <int NP, int T, int KS> DI void gc_lda(const GC& c, GR& g) {
  if constexpr (T < NP) asm volatile("buffer_load_dwordx4 %0, %1, %2, %3 offen offset:%4 sc1" : "=v"(g.a[T % 3][KS]) : "v"(c.vo), "s"(c.rA), "s"(c.soA + T * 4096), "i"(KS * 1024) : "memory");
}
template <int NP, int HO, int N, int J> DI void gc_rd(const GC& c, GR& g) {
  if constexpr (N < 4 * NP) { LAS const char* b = c.bR[J] + ((N >> 2) & 3) * 16384 + (N & 3) * 4096; g.w[N & 1][J] = ldstr(b, b + HO); }
}
template <int NP, int LDB, int HO, int N, class X> DI void gc_step(const GC& c, GR& g, f32x16 (&o)[4], X& x) {
  constexpr int T = N >> 2, KS = N & 3;
  if constexpr (KS == 3 && T + 1 < NP) {
    x.template pre<T>();
    WAITVL((T > 0 && T + 3 < NP ? 2 : 0) + (T + 2 < NP ? 4 : 0) + X::extra(T)); BAR();
    asm volatile("" : "+v"(g.a[(T + 1) % 3][0]), "+v"(g.a[(T + 1) % 3][1]), "+v"(g.a[(T + 1) % 3][2]), "+v"(g.a[(T + 1) % 3][3])); SB();
  }
  const bf16x8 a = __builtin_bit_cast(bf16x8, g.a[T % 3][KS]);
  o[0] = MFMA(g.w[N & 1][0], a, o[0]); SB(); gc_rd<NP, HO, N + 1, 0>(c, g); SB();
  o[1] = MFMA(g.w[N & 1][1], a, o[1]); SB(); gc_rd<NP, HO, N + 1, 1>(c, g);
  if constexpr (KS == 3) gc_dma<NP, LDB, T + 4, 0>(c); else gc_lda<NP, T + 2, KS>(c, g);
  SB();
  o[2] = MFMA(g.w[N & 1][2], a, o[2]); SB(); gc_rd<NP, HO, N + 1, 2>(c, g); x.template at<N>(); SB();
  o[3] = MFMA(g.w[N & 1][3], a, o[3]); SB(); gc_rd<NP, HO, N + 1, 3>(c, g);
  if constexpr (KS == 3) gc_dma<NP, LDB, T + 4, 1>(c); else if constexpr (KS == 2) gc_lda<NP, T + 2, 3>(c, g);
  SB();
}
template <int NP, int LDB, int HO, int N, class X> DI void gc_run(const GC& c, GR& g, f32x16 (&o)[4], X& x) {
  if constexpr (N < 4 * NP) { gc_step<NP, LDB, HO, N>(c, g, o, x); gc_run<NP, LDB, HO, N + 1>(c, g, o, x); }
}
template <int NP, int LDB> DI void gc_dma4(const GC& c) {
  gc_dma<NP, LDB, 0, 0>(c); gc_dma<NP, LDB, 0, 1>(c); gc_dma<NP, LDB, 1, 0>(c); gc_dma<NP, LDB, 1, 1>(c);
  gc_dma<NP, LDB, 2, 0>(c); gc_dma<NP, LDB, 2, 1>(c); gc_dma<NP, LDB, 3, 0>(c); gc_dma<NP, LDB, 3, 1>(c);
}
template <int NP> DI void gc_lda2(const GC& c, GR& g) {
  gc_lda<NP, 0, 0>(c, g); gc_lda<NP, 0, 1>(c, g); gc_lda<NP, 0, 2>(c, g); gc_lda<NP, 0, 3>(c, g);
  gc_lda<NP, 1, 0>(c, g); gc_lda<NP, 1, 1>(c, g); gc_lda<NP, 1, 2>(c, g); gc_lda<NP, 1, 3>(c, g);
}
template <int NP, int HO> DI void gc_first(const GC& c, GR& g) {
  asm volatile("" : "+v"(g.a[0][0]), "+v"(g.a[0][1]), "+v"(g.a[0][2]), "+v"(g.a[0][3]));
  gc_rd<NP, HO, 0, 0>(c, g); gc_rd<NP, HO, 0, 1>(c, g); gc_rd<NP, HO, 0, 2>(c, g); gc_rd<NP, HO, 0, 3>(c, g); SB();
}

constexpr int LDSZ = 163840;
constexpr float QSCALE = 0.08838834764831845f * 1.4426950408889634f;
struct K1C {
  rsrc_t rA, rW0, rW1, rX, rI, rK;
  unsigned voA[2], voB, voI[2], vo, m0A, m0B, soA, soB, soX, soK; int rot;
  LAS const char* aR[4]; LAS const char* bR;
  rsrc_t rP, rN; unsigned voP, voT, soP, voN, soN, voF; u32x4 xn; FL fl;
};
constexpr int NPRO = 8, QUO = 4;
struct K1Op { int kind, item, piece; };
constexpr int k1_xslot(int i) { return i % 5 == 0 ? 6 : i % 5 == 1 ? 7 : i == 2 ? 4 : i == 3 ? 5 : i == 4 ? 9 : 4; }
constexpr int k1_wslot(int i) { return i % 5 == 0 ? 2 : i % 5 == 1 ? 3 : i == 3 ? 0 : i == 4 ? 1 : 8; }
constexpr K1Op k1_free(int p, int i) {
  if (p < 0) { const int q = p + 10; constexpr int it[10] = {0, 0, 1, 1, 2, 3, 2, 4, 5, 3}; return i < 2 ? K1Op{q % 3 == 0 ? 2 : 1, it[q], i} : K1Op{0, 0, 0}; }
  if (i < 2) return p < 26 ? K1Op{1, p + 6, i} : p == 26 ? K1Op{4, 0, i} : p == 27 ? K1Op{4, 1, i} : p == 28 ? K1Op{3, 2, i} : p == 29 ? K1Op{3, 3, i} : p == 30 ? K1Op{4, 3, i}
                  : p == 31 ? K1Op{4, 4, i} : p < 35 ? K1Op{3, p - 27, i} : K1Op{0, 0, 0};
  if (i == 2) return p == 0 ? K1Op{0, 0, 0} : p < 25 ? K1Op{2, (p - 1) / 2 + 4, (p - 1) & 1} : p < 29 ? K1Op{3, (p - 25) / 2, (p - 1) & 1} : p < 31 ? K1Op{4, 2, (p - 1) & 1}
                   : p == 31 ? K1Op{3, 4, 0} : p < 35 ? K1Op{4, p - 27, 0} : K1Op{0, 0, 0};
  return p == 31 ? K1Op{3, 4, 1} : p >= 32 && p < 35 ? K1Op{4, p - 27, 1} : K1Op{0, 0, 0};
}
constexpr int k1_cum(int p) {
  int n = 0;
  for (int q = -10; q <= p; ++q) for (int i = 0; i < 4; ++i) n += k1_free(q, i).kind != 0;
  return p < -1 ? 0 : n < NPRO + QUO * (p + 1) ? n : NPRO + QUO * (p + 1);
}
constexpr K1Op k1_seq(int n) {
  for (int q = -10; q < 40; ++q) for (int i = 0; i < 4; ++i) if (k1_free(q, i).kind && n-- == 0) return k1_free(q, i);
  return K1Op{0, 0, 0};
}
constexpr K1Op k1_op(int p, int i) {
  if (p < 0 || i < 4) return k1_cum(p - 1) + i < k1_cum(p) ? k1_seq(k1_cum(p - 1) + i) : p < 0 && i == 19 ? K1Op{9, 0, 0} : K1Op{0, 0, 0};
  if (i == 4) return p == 0 ? K1Op{7, 0, 0} : p >= 16 && p < 31 && !(p & 1) ? K1Op{5, 0, (p - 16) / 2} : p == 32 || p == 33 ? K1Op{5, 1, p - 32} : p >= 34 && p < 37 ? K1Op{5, 1, 2 * p - 66} : K1Op{0, 0, 0};
  return p >= 34 && p < 37 ? K1Op{5, 1, 2 * p - 65} : p == 17 ? K1Op{8, 0, 0} : p == 37 ? K1Op{6, 0, 0} : p == 9 ? K1Op{6, 1, 0} : K1Op{0, 0, 0};
}
constexpr bool k1_needs(int p, K1Op o) {
  return p < 32 ? (o.kind == 1 && o.item == p) || (o.kind == 2 && o.item == p / 2) : (o.kind == 3 || o.kind == 4) && o.item == p - 32;
}
constexpr int k1_wait(int p, int upto) {
  int n = 0, got = 0;
  for (int q = -1; q < upto; ++q) for (int i = 0; i < (q < 0 ? 20 : 6); ++i) { const K1Op o = k1_op(q, i); if (k1_needs(p, o)) { n = 0; ++got; } else if (o.kind) ++n; }
  return got == 4 ? n : -1;
}
struct K1P { unsigned t; u32x2 f; };
struct K1A { f32x16 k[2], v[2]; };
template <int P, int I> DI void k1_vm(const K1C& c, const K1A& a0, const K1A& a1, K1P& pf) {
  constexpr K1Op o = k1_op(P, I);
  if constexpr (o.kind == 1) dma16<0>(o.piece ? c.rW1 : c.rW0, c.voB, c.soB + ((o.item + 2 * c.rot) & 15) * 262144, c.m0B + (o.item % 6) * 16384 + o.piece * 8192);
  if constexpr (o.kind == 2) dma16<0>(c.rA, c.voA[o.piece], c.soA + (o.item >> 3) * 131072 + ((o.item + c.rot) & 7) * 128, c.m0A + (6 + (o.item & 3)) * 16384 + o.piece * 1024);
  if constexpr (o.kind == 3) dma16<0>(c.rX, c.voA[o.piece], c.soX + ((o.item + c.rot) & 7) * 128, c.m0A + k1_xslot(o.item) * 16384 + o.piece * 1024);
  if constexpr (o.kind == 4) dma16<0>(c.rI, c.voI[o.piece], ((o.item + c.rot) & 7) * (64 * FI * 2), c.m0A + k1_wslot(o.item) * 16384 + o.piece * 1024);
  if constexpr (o.kind == 5) {
    const K1A& a = o.item ? a1 : a0;
    const f32x16& t = o.piece & 2 ? a.v[o.piece >> 2] : a.k[o.piece >> 2];
    bst16(pack8<o.piece & 1>(t), c.rK, c.vo, c.soK + ((o.piece >> 1) & 1) * (16u << 20) + o.item * 32768 + (o.piece >> 2) * 8192 + (o.piece & 1) * 1024);
  }
  if constexpr (o.kind == 7) bst16(c.xn, c.rN, c.voN, c.soN);
  if constexpr (o.kind == 8) fl_ld(c.fl, pf.f, c.voF);
  if constexpr (o.kind == 6) asm volatile("buffer_load_dword %0, %1, %2, %3 offen" : "+v"(pf.t) : "v"(o.item ? c.voT : c.voP), "s"(o.item ? c.rI : c.rP), "s"(o.item ? 0u : c.soP) : "memory");
}
struct K1F { bf16x8 a[2][2], w[2][2]; };
template <int P, int G> DI void k1_rd(const K1C& c, K1F& f) {
  if constexpr (P < 32) {
    constexpr int KS = G / 3;
    if constexpr (G % 3 == 1) { LAS const char* a = c.aR[2 * (P & 1) + KS] + ((P >> 1) & 3) * 16384; f.a[KS][0] = ldsr(a); f.a[KS][1] = ldsr(a + 4096); }
    else { LAS const char* b = c.bR + (P % 6) * 16384 + KS * 4096 + (G % 3 / 2) * 8192; f.w[KS][G % 3 / 2] = ldstr(b, b + 1024); }
  }
}
struct K1Z { bf16x8 x[2], w[2][3], d[2]; };
struct K1ZA { LAS const char* xR[2]; LAS const char* wR; LAS const char* dR; f32x16 acc[3]; float s, q, cs; };
template <int S, int K, int G> DI void k1z_rd(const K1ZA& z, K1Z& f) {
  if constexpr (S < 8) {
    constexpr int o = k1_wslot(S) * 16384 + K * 16 * 192;
    f.w[K][G] = ldstr(z.wR + o + G * 64, z.wR + o + G * 64 + 768);
    if constexpr (G == 0) f.x[K] = ldsr(z.xR[K] + k1_xslot(S) * 16384);
    if constexpr (G == 1) f.d[K] = ldstr(z.dR + o, z.dR + o + 768);
  }
}
template <int P> DI void k1_per(const K1C& c, const K1F& f, K1F& fn, K1A& a0, K1A& a1, K1P& pf, const K1ZA& z, K1Z& g) {
  K1A& a = P < 16 ? a0 : a1;
  static_assert(k1_wait(P + 1, P) >= 0, "operand not requested");
  WAITVL(k1_wait(P + 1, P)); BAR(); SB();
#define KM(ks, i) a.k[i] = MFMA(f.w[ks][0], f.a[ks][i], a.k[i]); SB()
#define VM(ks, i) a.v[i] = MFMA(f.a[ks][i], f.w[ks][1], a.v[i]); SB()
#define KR(G) if constexpr (P < 31) k1_rd<P + 1, G>(c, fn); else k1z_rd<0, (G) / 3, (G) % 3>(z, g)
  if constexpr (P == 8) fl_pub(c.fl, F_XN + blockIdx.x * 8, c.soB < 32768 && c.vo == 0);
  KM(0, 0); KR(0); SB(); KM(0, 1); KR(1); SB(); VM(0, 0); KR(2); SB();
  VM(0, 1); KR(3); k1_vm<P, 0>(c, a0, a1, pf); SB(); KM(1, 0); KR(4); k1_vm<P, 1>(c, a0, a1, pf); SB(); KM(1, 1); KR(5); k1_vm<P, 2>(c, a0, a1, pf); SB();
  VM(1, 0); k1_vm<P, 3>(c, a0, a1, pf); SB(); VM(1, 1); k1_vm<P, 4>(c, a0, a1, pf); k1_vm<P, 5>(c, a0, a1, pf);
  if constexpr (P == 24) { if (c.soB < 32768) fl_chk<false>(c.fl, pf.f, c.voF); }
  SB();
}
template <int P, int E> DI void k1_run(const K1C& c, K1F& f0, K1F& f1, K1A& a0, K1A& a1, K1P& pf, const K1ZA& z, K1Z& g) {
  if constexpr (P < E) { k1_per<P>(c, f0, f1, a0, a1, pf, z, g); k1_per<P + 1>(c, f1, f0, a0, a1, pf, z, g); k1_run<P + 2, E>(c, f0, f1, a0, a1, pf, z, g); }
}
template <int S, int K> DI void k1z_half(const K1C& c, const K1A& a1, K1ZA& z, const K1Z& f, K1Z& fn, K1P& pf) {
  const u32x4 x = __builtin_bit_cast(u32x4, f.x[K]), d = __builtin_bit_cast(u32x4, f.d[K]);
  z.acc[0] = MFMA(f.w[K][0], f.x[K], z.acc[0]); SB(); k1z_rd<S + 1, K, 0>(z, fn);
#pragma unroll
  for (int e = 0; e < 4; ++e) z.s = dot2(x[e], 0x3f803f80u, z.s);
  k1_vm<32 + S, 3 * K>(c, a1, a1, pf); SB();
  z.acc[1] = MFMA(f.w[K][1], f.x[K], z.acc[1]); SB(); k1z_rd<S + 1, K, 1>(z, fn);
#pragma unroll
  for (int e = 0; e < 4; ++e) z.q = dot2(x[e], x[e], z.q);
  k1_vm<32 + S, 3 * K + 1>(c, a1, a1, pf); SB();
  z.acc[2] = MFMA(f.w[K][2], f.x[K], z.acc[2]); SB(); k1z_rd<S + 1, K, 2>(z, fn);
#pragma unroll
  for (int e = 0; e < 4; ++e) z.cs = dot2(d[e], 0x3f803f80u, z.cs);
  k1_vm<32 + S, 3 * K + 2>(c, a1, a1, pf); SB();
}
template <int S> DI void k1z_stage(const K1C& c, const K1A& a1, K1ZA& z, const K1Z& f, K1Z& fn, K1P& pf) {
  if constexpr (S < 7) { WAITVL(k1_wait(33 + S, 32 + S)); BAR(); SB(); }
  k1z_half<S, 0>(c, a1, z, f, fn, pf); k1z_half<S, 1>(c, a1, z, f, fn, pf);
}
template <int KH> DI void k1z_send(LAS f32x4* ex, const f32x16 (&acc)[3], f32x4 (&own)[3][2]) {
#pragma unroll
  for (int nb = 0; nb < 3; ++nb)
#pragma unroll
    for (int e = 0; e < 2; ++e) {
      constexpr int o = 8 * KH, t = 8 - o;
      ex[(nb * 2 + e) * 64] = (f32x4){acc[nb][t + 4 * e], acc[nb][t + 4 * e + 1], acc[nb][t + 4 * e + 2], acc[nb][t + 4 * e + 3]};
      own[nb][e] = (f32x4){acc[nb][o + 4 * e], acc[nb][o + 4 * e + 1], acc[nb][o + 4 * e + 2], acc[nb][o + 4 * e + 3]};
      asm volatile("" : "+v"(own[nb][e]));
    }
}
struct ZE {
  LAS const f32x4* ex; LAS const float* cs; f32x4 own[3][2]; float rs, mrs; rsrc_t rz; unsigned vo; int k16, rb32;
  template <int NB> DI void gelu() const {
    u32x4 w;
#pragma unroll
    for (int e = 0; e < 2; ++e) {
      const f32x4 c0 = *(LAS const f32x4*)(cs + NB * 32 + 8 * e), c1 = *(LAS const f32x4*)(cs + 128 + NB * 32 + 8 * e);
      const f32x4 t = (ex[(NB * 2 + e) * 64] + own[NB][e]) * rs + (c0 + c1) * mrs;
      const f32x4 u = t * (t * t * -0.1029432397f - 2.302208198f);
      f32x4 d;
#pragma unroll
      for (int cc = 0; cc < 4; ++cc) d[cc] = __builtin_amdgcn_exp2f(u[cc]);
      d += 1.f;
#pragma unroll
      for (int cc = 0; cc < 4; ++cc) d[cc] = __builtin_amdgcn_rcpf(d[cc]);
      d *= t;
      w[2 * e] = cvtpk(d[0], d[1]); w[2 * e + 1] = cvtpk(d[2], d[3]);
    }
    const int k = k16 + 2 * NB;
    if (k < 43) bst16(w, rz, vo, (rb32 * 43 + k) * 1024);
    else bst16(natural8(w), rz, k < 86 ? vo : 0x80000000u, SEQ * FH * 2 + (rb32 * 43 + k - 43) * 1024);
  }
};
DI void p1_kvz(LAS char* lds, FL& fl, GC& q, GR& qr, ZE& ze) {
  const KA ka = kargs();
  char* ws = ka->ws;
  const int tid = threadIdx.x, lane = tid & 63, wid = rfl(tid >> 6), r = lane & 31, h = lane >> 5;
  const int bid = blockIdx.x, xcd = bid & 7, idx = bid >> 3, head = xcd * 4 + (idx >> 3), rb = idx & 7;
  const int wm = wid >> 2, wn = wid & 3, q4 = (lane >> 2) & 3, tt = h ^ ((r >> 1) & 7);
  const int zrb = (xcd >> 1) * 4 + (idx >> 3), cg = (xcd & 1) * 8 + rb;
  TSD; TS(0);
  K1C c;
  K1A a0 = {}, a1 = {};
  K1P pf = {};
  const unsigned ldsb = (unsigned)(size_t)lds;
  u32x4 xv;
  c.rX = mkrsrc(ka->x, SEQ * DM * 2);
  asm volatile("buffer_load_dwordx4 %0, %1, %2, %3 offen" : "=v"(xv) : "v"(lane * 16), "s"(c.rX), "s"((bid * 8 + wid) * 1024) : "memory");
  c.rA = mkrsrc(ka->ckv, SEQ * DM * 2); c.rW0 = mkrsrc(ka->wk, DM * 4096 * 2); c.rW1 = mkrsrc(ka->wv, DM * 4096 * 2);
#pragma unroll
  for (int p = 0; p < 2; ++p) c.voA[p] = (8 * p + (lane >> 3)) * 1024 + (((lane & 7) ^ (4 * p + (lane >> 4))) * 16);
  c.voB = (lane >> 4) * 8192 + (((lane & 15) ^ ((lane >> 4) << 2)) * 16);
  c.soA = (rb * 256 + wid * 16) * 1024; c.soB = wid * 4 * 8192 + head * 256;
  c.rot = (rb + 2 * (idx >> 3)) & 7;
  c.m0A = ldsb + wid * 2048; c.m0B = ldsb + wid * 1024;
#define P2(q) k1_vm<-1, q>(c, a0, a1, pf); k1_vm<-1, q + 1>(c, a0, a1, pf)
  P2(0); P2(2); P2(4); P2(6); P2(8); P2(10); P2(12); P2(14); P2(16); P2(18);
#pragma unroll
  for (int ks = 0; ks < 4; ++ks) c.aR[ks] = lds + 98304 + (wm * 64 + r) * 128 + ((tt ^ (2 * ks)) * 16);
  c.bR = lds + (8 * h + q4) * 256 + ((wn ^ q4) * 64) + ((lane >> 4) & 1) * 32 + (lane & 3) * 8;
  c.rI = mkrsrc(ka->w_in, DM * FI * 2); c.rK = mkrsrc(ws + WS_K, 32u << 20);
  c.soX = (zrb * 128 + wid * 16) * 1024; c.vo = lane * 16;
  c.soK = ((head * 64 + rb * 8 + wm * 2) * 8 + 2 * wn) * 1024;
  c.voF = F_XN + ((lane & 16 ? rb : 15 - rb) * 16 + (lane & 15)) * 8;
#pragma unroll
  for (int i = 0; i < 2; ++i) {
    const int o = (wid * 2 + i) * 64 + lane;
    c.voI[i] = wid < 6 && cg < 15 ? (o / 12) * (FI * 2) + (o % 12) * 16 + cg * 192 : 0x80000000u;
  }
  c.rP = mkrsrc(ka->wq, DM * 4096 * 2); c.voP = r * 64; c.voT = lane < 11 ? ((bid * 8 + wid) * 11 + lane) * 64 : 0x80000000u; c.soP = (bid * 8 + wid) * 2048;
  const int wr = wid & 3, kh = wid >> 2;
  K1ZA z; K1Z g0, g1;
#pragma unroll
  for (int k = 0; k < 2; ++k) z.xR[k] = lds + (wr * 32 + r) * 128 + ((tt ^ (2 * (2 * kh + k))) * 16);
  z.wR = lds + (32 * kh + 8 * h + q4) * 192 + ((lane >> 4) & 1) * 32 + (lane & 3) * 8;
  z.dR = z.wR + (wr % 3) * 64;
  {
    const int w = idx * 8 + wid, i = w < 64 ? 10 : ((w - 64) >> 2) % 12;
    const unsigned sz = i == 10 ? 122u << 20 : i == 7 ? 8u << 20 : i >= 2 && i <= 5 ? 4u << 20 : i == 8 ? 4096u : i == 9 ? FH * DM * 2 : i == 6 ? DM * FI * 2 : 2u << 20;
    const char* base = ((const char* const __attribute__((address_space(4)))*)ka)[i];
    asm volatile("buffer_load_dword %0, %1, %2, 0 offen" : "+v"(pf.t) : "v"((unsigned)(w < 64 ? w : w & 3) << 21), "s"(mkrsrc(base, sz)) : "memory");
  }
  unsigned long long ep;
  asm volatile("s_load_dwordx2 %0, %1, 0" : "=s"(ep) : "s"(ws + WS_F) : "memory");
  TS(1);
  WAITVL(NPRO + 1); SB();
  asm volatile("" : "+s"(ep));
  fl.r = mkrsrc(ws + WS_F, 262144); fl.lo = (unsigned)(ep + 1); fl.hi = (unsigned)((ep + 1) >> 32); c.fl = fl;
  {
    asm volatile("" : "+v"(xv));
    float s = 0.f, q = 0.f;
#pragma unroll
    for (int e = 0; e < 4; ++e) { s = dot2(xv[e], 0x3f803f80u, s); q = dot2(xv[e], xv[e], q); }
#pragma unroll
    for (int o = 1; o < 32; o <<= 1) { s += __shfl_xor(s, o); q += __shfl_xor(q, o); }
    s = swap32_add(s); q = swap32_add(q);
    float mb, rs; ln_stats(s, q, 1.f / DM, mb, rs);
#pragma unroll
    for (int e = 0; e < 4; ++e) c.xn[e] = cvtpk((bflo(xv[e]) - mb) * rs, (bfhi(xv[e]) - mb) * rs);
    *(LAS u32x4*)(lds + 147456 + lane * 128 + wid * 16) = c.xn;
    static_assert(k1_seq(18).kind == 2 && k1_seq(18).item == 3 && k1_cum(-1) <= 18, "slot 9");
    c.rN = mkrsrc(ws + WS_XN, SEQ * DM * 2); c.voN = (lane >> 4) * 1024 + ((lane >> 3) & 1) * 512 + (lane & 7) * 16; c.soN = (bid >> 2) * 32768 + wid * 4096 + (bid & 3) * 128;
  }
  K1F f0, f1;
  SB(); WAITVL(k1_wait(0, 0)); BAR(); SB();
  TS(2);
  c.xn = *(LAS const u32x4*)(lds + 147456 + wid * 1024 + lane * 16);
  k1_rd<0, 0>(c, f0); k1_rd<0, 1>(c, f0); k1_rd<0, 2>(c, f0); k1_rd<0, 3>(c, f0); k1_rd<0, 4>(c, f0); k1_rd<0, 5>(c, f0); SB();
  k1_run<0, 8>(c, f0, f1, a0, a1, pf, z, g0);
  TS(3);
  k1_run<8, 16>(c, f0, f1, a0, a1, pf, z, g0);
  TS(4);
  k1_run<16, 24>(c, f0, f1, a0, a1, pf, z, g0);
  TS(5);
  k1_run<24, 32>(c, f0, f1, a0, a1, pf, z, g0);
  TS(6);
  z.acc[0] = (f32x16){}; z.acc[1] = (f32x16){}; z.acc[2] = (f32x16){}; z.s = 0.f; z.q = 0.f; z.cs = 0.f;
  k1z_stage<0>(c, a1, z, g0, g1, pf); k1z_stage<1>(c, a1, z, g1, g0, pf); k1z_stage<2>(c, a1, z, g0, g1, pf); k1z_stage<3>(c, a1, z, g1, g0, pf);
  k1z_stage<4>(c, a1, z, g0, g1, pf); k1z_stage<5>(c, a1, z, g1, g0, pf); k1z_stage<6>(c, a1, z, g0, g1, pf); k1z_stage<7>(c, a1, z, g1, g0, pf);
  TS(7);
  {
    LAS f32x4* ex = (LAS f32x4*)lds;
    LAS f32x2* st = (LAS f32x2*)(lds + 49152);
    LAS float* cs = (LAS float*)(lds + 51200);
    if (kh) k1z_send<1>(ex + wid * 384 + lane, z.acc, ze.own); else k1z_send<0>(ex + wid * 384 + lane, z.acc, ze.own);
    { const float s = swap32_add(z.s), q = swap32_add(z.q), cc = swap32_add(z.cs); st[wid * 32 + r] = (f32x2){s, q}; cs[wid * 32 + r] = cc; }
    WAITL0(); BAR(); SB();
    q.rA = mkrsrc(ws + WS_XN, SEQ * DM * 2); q.rB = c.rP;
    q.vo = c.vo; q.voB[0] = c.voB; q.voB[1] = c.voB + 31744;
    q.soA = ((kh ? rb : 15 - rb) * 4 + wr) * 32768; q.soB = wid * 65536 + head * 256; q.m0 = ldsb + 65536 + wid * 2048;
    gc_dma<8, 8192, 0, 0>(q); gc_dma<8, 8192, 0, 1>(q); gc_lda<8, 0, 0>(q, qr); gc_lda<8, 0, 1>(q, qr); gc_lda<8, 0, 2>(q, qr); gc_lda<8, 0, 3>(q, qr);
    gc_dma<8, 8192, 1, 0>(q); gc_dma<8, 8192, 1, 1>(q); gc_lda<8, 1, 0>(q, qr); gc_lda<8, 1, 1>(q, qr); gc_lda<8, 1, 2>(q, qr); gc_lda<8, 1, 3>(q, qr);
    SB();
    const f32x2 s0 = st[wr * 32 + r], s1 = st[(wr + 4) * 32 + r];
    float mb; ln_stats(s0[0] + s1[0], s0[1] + s1[1], 1.f / DM, mb, ze.rs);
    ze.mrs = -mb * ze.rs; ze.rz = mkrsrc(ws + WS_Z1, 2 * SEQ * FH * 2);
    ze.ex = ex + (wid ^ 4) * 384 + lane; ze.cs = cs + 16 * kh + 4 * h; ze.vo = c.vo; ze.k16 = 6 * cg + kh; ze.rb32 = zrb * 4 + wr;
    ze.gelu<0>();
    SB();
    gc_dma<8, 8192, 2, 0>(q); gc_dma<8, 8192, 2, 1>(q); gc_dma<8, 8192, 3, 0>(q); gc_dma<8, 8192, 3, 1>(q);
  }
  TS(8);
  asm volatile("" ::"v"(pf.t));
  TSW(1);
}

constexpr int TCH = 2;
constexpr float THR = 32.f;
struct K2C {
  rsrc_t rK, rV, rC;
  unsigned vo, ldsb;
  unsigned mS, mP;
  int n1, g, qrow;
  int rowA, rowB;
};
DI int k2_tile(const K2C& c, int n) { n = n > 16 ? 16 : n; return n < c.n1 ? n : 2 * n - c.n1 + c.g; }
DI unsigned k2_so(const K2C& c, int n) { return (n < c.n1 ? c.mS : (c.mP & 0xffffu)) + (unsigned)k2_tile(c, n) * 16384u; }
DI unsigned k2_m0(const K2C& c, int n) { return c.ldsb + (n < c.n1 ? c.mS : c.mP); }
DI LAS const char* k2_rd(const K2C& c, int n) { return (LAS const char*)(size_t)(c.vo + (c.ldsb + (n < c.n1 ? 0u : (c.mP & 0x10000u)))); }
template <int IMM> DI void dma16c(rsrc_t r, unsigned voff, unsigned soff, int a, int b) {
  asm volatile("s_cmp_lt_i32 %4, %5\n\ts_cbranch_scc1 .Ld%=\n\tbuffer_load_dwordx4 %0, %1, %2 offen offset:%3 lds\n.Ld%=:" ::"v"(voff), "s"(r), "s"(soff), "i"(IMM), "s"(a), "s"(b) : "scc", "memory");
}
DI void k2_mask(f32x16 (&s)[2], int dq) {
#pragma unroll
  for (int kb = 0; kb < 2; ++kb)
#pragma unroll
    for (int e = 0; e < 16; ++e) { const int cc = 32 * kb + (e & 3) + 8 * (e >> 2); if (cc > dq) s[kb][e] = -__builtin_inff(); }
}
DI float max3(float a, float b, float c) { float d; asm("v_max3_f32 %0, %1, %2, %3" : "=v"(d) : "v"(a), "v"(b), "v"(c)); return d; }
DI float k2_max(const f32x16 (&s)[2]) {
  float m0 = fmaxf(s[0][0], s[0][1]), m1 = fmaxf(s[1][0], s[1][1]);
#pragma unroll
  for (int e = 2; e < 16; e += 2) { m0 = fmaxf(fmaxf(m0, s[0][e]), s[0][e + 1]); m1 = fmaxf(fmaxf(m1, s[1][e]), s[1][e + 1]); }
  return fmaxf(m0, m1);
}
DI void addf(float& a, float b) { asm("v_add_f32 %0, %0, %1" : "+v"(a) : "v"(b)); }
DI void k2_loadq(u32x4 (&q)[8], unsigned la, int a, int b) {
  asm volatile("s_cmp_lg_u32 %9, %10\n\ts_cbranch_scc1 .Lq%=\n\t"
               "ds_read_b128 %0, %8\n\tds_read_b128 %1, %8 offset:1024\n\tds_read_b128 %2, %8 offset:2048\n\tds_read_b128 %3, %8 offset:3072\n\t"
               "ds_read_b128 %4, %8 offset:4096\n\tds_read_b128 %5, %8 offset:5120\n\tds_read_b128 %6, %8 offset:6144\n\tds_read_b128 %7, %8 offset:7168\n"
               ".Lq%=:" : "+v"(q[0]), "+v"(q[1]), "+v"(q[2]), "+v"(q[3]), "+v"(q[4]), "+v"(q[5]), "+v"(q[6]), "+v"(q[7])
               : "v"(la), "s"(a), "s"(b) : "scc", "memory");
}
struct K2S { f32x16 o[4], negm; u32x4 q[8]; bf16x8 f[3]; LAS const char* rk; float m, l; };

DI void k2_rescale(K2S& S, f32x16 (&sn)[2], float lmax, bool first) {
  float dl = swap32_max(lmax);
  if (first) { dl = dl == -__builtin_inff() ? 0.f : dl; S.m = dl; }
  else {
    dl = fmaxf(dl, 0.f); S.m += dl;
    const float f = __builtin_amdgcn_exp2f(-dl);
    S.l *= f;
#pragma unroll
    for (int db = 0; db < 4; ++db) S.o[db] *= f;
  }
#pragma unroll
  for (int kb = 0; kb < 2; ++kb) sn[kb] -= dl;
#pragma unroll
  for (int e = 0; e < 16; ++e) S.negm[e] = -S.m;
}
DI void k2_finishB(const K2C& c, K2S& S, unsigned ctx) {
  const float inv = __builtin_amdgcn_rcpf(swap32_add(S.l));
#pragma unroll
  for (int db = 0; db < 4; ++db) {
    const f32x16 res = S.o[db] * inv;
    bst16(pack8<0>(res), c.rC, c.vo, ctx + db * 2048); bst16(pack8<1>(res), c.rC, c.vo, ctx + db * 2048 + 1024);
    S.o[db] = (f32x16){};
  }
  S.l = 0.f;
}
DI void k2_finishA(const K2C& c, K2S& S, LAS char* lds, int wid, unsigned ctx) {
  const int lane = c.vo >> 4;
  const int g = wid >> 2, pw = wid ^ 4;
  LAS f32x4* ex = (LAS f32x4*)lds;
  LAS f32x2* es = (LAS f32x2*)(lds + 65536);
  const float lt = swap32_add(S.l);
  const float me = lt > 0.f ? S.m : -1e30f;
  es[wid * 64 + lane] = (f32x2){me, lt};
#pragma unroll
  for (int rd = 0; rd < 2; ++rd) {
    const f32x16 snd = g ? S.o[rd] : S.o[2 + rd];
#pragma unroll
    for (int e = 0; e < 4; ++e) ex[(wid * 8 + rd * 4 + e) * 64 + lane] = (f32x4){snd[4 * e], snd[4 * e + 1], snd[4 * e + 2], snd[4 * e + 3]};
  }
  WAITL0(); BAR();
  const f32x2 ps = es[pw * 64 + lane];
  const float mm = fmaxf(me, ps[0]);
  float w0 = __builtin_amdgcn_exp2f(me - mm), w1 = __builtin_amdgcn_exp2f(ps[0] - mm);
  const float inv = __builtin_amdgcn_rcpf(w0 * lt + w1 * ps[1]);
  w0 *= inv; w1 *= inv;
#pragma unroll
  for (int rd = 0; rd < 2; ++rd) {
    const f32x16 own = g ? S.o[2 + rd] : S.o[rd];
    f32x16 res;
#pragma unroll
    for (int e = 0; e < 4; ++e) {
      const f32x4 v = ex[(pw * 8 + rd * 4 + e) * 64 + lane];
#pragma unroll
      for (int c = 0; c < 4; ++c) res[4 * e + c] = own[4 * e + c] * w0 + v[c] * w1;
    }
    const unsigned p = ctx + (2 * g + rd) * 2048;
    bst16(pack8<0>(res), c.rC, c.vo, p); bst16(pack8<1>(res), c.rC, c.vo, p + 1024);
  }
}
template <int PAR> DI bf16x8 k2_frag(LAS const char* rd, int i) {
  if (i < 16) return ldsr(rd + (PAR ^ 1) * 16384 + i * 1024);
  if (i >= 32) return ldsr(rd + PAR * 16384 + (i - 32) * 1024);
  const int ks = (i - 16) >> 2, db = i & 3;
  return ldsr(rd + 32768 + PAR * 16384 + (ks >> 1) * 8192 + db * 2048 + (ks & 1) * 1024);
}
template <int PAR, bool NEXT> DI void k2_step(const K2C& c, K2S& S, int n, f32x16 (&sc)[2], f32x16 (&sn)[2], unsigned qnext, unsigned ctxB) {
  bf16x8 F[35];
  F[0] = S.f[0]; F[1] = S.f[1]; F[2] = S.f[2];
  u32x4 p[4];
  float l0 = S.l, l1 = 0.f, lmax = 0.f;
  unsigned soK = 0, soV = 0, soK2 = 0;
  LAS const char* rv = nullptr;
  if constexpr (NEXT) { soV = k2_so(c, n + 1); soK = k2_so(c, n + 3); soK2 = k2_so(c, n + 2); }
#pragma unroll
  for (int s = 0; s < 32; ++s) {
    if (s == 29) { asm volatile("s_waitcnt vmcnt(0) lgkmcnt(0)" ::: "memory"); BAR(); SB(); }
    if (s < 16) { if constexpr (NEXT) sn[s >> 3] = MFMA(F[s], __builtin_bit_cast(bf16x8, S.q[s & 7]), (s & 7) == 0 ? S.negm : sn[s >> 3]); }
    else S.o[s & 3] = MFMA(F[s], __builtin_bit_cast(bf16x8, p[(s - 16) >> 2]), S.o[s & 3]);
    SB();
    if (s == 0 && NEXT) F[3] = k2_frag<PAR>(S.rk, 3);
    if (s <= 27 && (NEXT || s >= 12)) F[s + 4] = k2_frag<PAR>(s < 12 ? S.rk : rv, s + 4);
    if (s >= 29 && NEXT) F[s + 3] = k2_frag<PAR>(S.rk, s + 3);
    if (s == 9) rv = k2_rd(c, n);
    if (s == 18 && NEXT) S.rk = k2_rd(c, n + 2);
    const int e0 = s < 5 ? 2 * s : s + 5, e1 = s < 5 ? 2 * s + 2 : s + 6;
    const int a0 = s < 6 ? 2 * s - 2 : s + 4, a1 = s < 6 ? 2 * s : s + 5;
    if (s <= 26) {
#pragma unroll
      for (int e = e0; e < e1; ++e) sc[e >> 4][e & 15] = __builtin_amdgcn_exp2f(sc[e >> 4][e & 15]);
    }
    if (s >= 1 && s <= 27) {
#pragma unroll
      for (int e = a0; e < a1; ++e) {
        if (e & 1) { addf(l1, sc[e >> 4][e & 15]); p[e >> 3][(e >> 1) & 3] = cvtpk(sc[e >> 4][(e & 15) - 1], sc[e >> 4][e & 15]); }
        else addf(l0, sc[e >> 4][e & 15]);
      }
    }
    if constexpr (NEXT) {
      if (s == 10) lmax = max3(sn[0][0], sn[0][1], sn[0][1]);
      if (s > 10 && s <= 17) lmax = max3(lmax, sn[0][2 * (s - 10)], sn[0][2 * (s - 10) + 1]);
      if (s >= 19 && s <= 26) lmax = max3(lmax, sn[1][2 * (s - 19)], sn[1][2 * (s - 19) + 1]);
      if (s == 1) dma16c<2048>(c.rK, c.vo, soK2, n + 2, c.n1);
      if (s == 3) dma16c<3072>(c.rK, c.vo, soK2, n + 2, c.n1);
      if (s == 5) dma16<0>(c.rV, c.vo, soV, k2_m0(c, n + 1) + 32768 + (PAR ^ 1) * 16384);
      if (s == 7) dma16n<1024>(c.rV, c.vo, soV);
      if (s == 9) dma16c<2048>(c.rV, c.vo, soV, n + 1, c.n1);
      if (s == 11) dma16c<3072>(c.rV, c.vo, soV, n + 1, c.n1);
      if (s == 16) k2_loadq(S.q, qnext, n + 2, c.g ? c.n1 : -1);
      if (s == 29) { dma16<0>(c.rK, c.vo, soK, k2_m0(c, n + 3) + (PAR ^ 1) * 16384); }
      if (s == 31) { dma16n<1024>(c.rK, c.vo, soK); }
    }
    if (s == 28) { S.l = l0 + l1; asm volatile("" : "+v"(S.l), "+v"(lmax)); }
    SB();
  }
  if constexpr (NEXT) {
    S.f[0] = F[32]; S.f[1] = F[33]; S.f[2] = F[34];
    const bool first = c.g && n + 1 == c.n1;
    const int kb = k2_tile(c, n + 1) * 64, rw = n + 1 < c.n1 ? c.rowB : c.rowA;
    if (kb + 63 > rw) { k2_mask(sn, rw + c.qrow - kb); lmax = k2_max(sn); }
    if (first || __any(lmax > THR)) k2_rescale(S, sn, lmax, first);
    if (first) k2_finishB(c, S, ctxB);
    if (c.g && n + 2 == c.n1) {
#pragma unroll
      for (int e = 0; e < 16; ++e) S.negm[e] = 0.f;
    }
    SB();
  }
}
DI void ld16(u32x4& v, rsrc_t r, unsigned vo, unsigned so) { asm volatile("buffer_load_dwordx4 %0, %1, %2, %3 offen sc1" : "=v"(v) : "v"(vo), "s"(r), "s"(so) : "memory"); }
struct QX {
  const FL& fl; const K2C& c; const ZE& ze; rsrc_t rz; unsigned voF, voZ, zo[2]; LAS char* kw; int bid, wid; u32x2 fk; u32x4 zv[2], kv[6];
  static constexpr int extra(int t) { return t == 1 ? 2 : t == 0 || t == 3 ? 6 : t == 5 ? 1 : 0; }
  template <int N> DI void at() {
    if constexpr (N == 4) fl_ld(fl, fk, voF);
    if constexpr (N < 8 && !(N & 1)) { if (c.g == ((N >> 1) & 1)) ze.gelu<1 + N / 4>(); }
    if constexpr (N == 11) fl_pub(fl, F_Z + bid * 8, wid == 0 && c.vo == 0);
    if constexpr (N == 20) fl_ld(fl, fk, voZ);
    if constexpr (N == 27) {
      fl_chk<false>(fl, fk, voZ); ld16(zv[0], rz, zo[0], 0); ld16(zv[1], rz, zo[1], 0);
      if (wid == 0) asm volatile("buffer_inv sc1" ::: "memory");
    }
    if constexpr (N >= 12 && N < 15) { ld16(kv[2 * N - 24], N == 14 ? c.rV : c.rK, c.vo, c.mS + (N == 13) * 16384); ld16(kv[2 * N - 23], N == 14 ? c.rV : c.rK, c.vo, c.mS + (N == 13) * 16384 + 1024); }
    if constexpr (N >= 20 && N < 23) {
      asm volatile("" : "+v"(kv[2 * N - 40]), "+v"(kv[2 * N - 39]));
      *(LAS u32x4*)(kw + (N - 20) * 16384) = kv[2 * N - 40]; *(LAS u32x4*)(kw + (N - 20) * 16384 + 1024) = kv[2 * N - 39];
    }
  }
  template <int T> DI void pre() { if constexpr (T == 2) { if (wid == 0) { asm volatile("s_waitcnt vmcnt(10)" : "+v"(fk)::"memory"); fl_chk<false>(fl, fk, voF); } } }
};
template <int N> DI void ln_part(const u32x4 (&v)[N], LAS f32x2* st, int wid, int lane) {
  float s = 0.f, q = 0.f;
#pragma unroll
  for (int i = 0; i < N; ++i)
#pragma unroll
    for (int e = 0; e < 4; ++e) { const float a = bflo(v[i][e]), b = bfhi(v[i][e]); s += a + b; q = fmaf(a, a, fmaf(b, b, q)); }
  s += __shfl_xor(s, 8); q += __shfl_xor(q, 8); s += __shfl_xor(s, 16); q += __shfl_xor(q, 16);
  s = swap32_add(s); q = swap32_add(q);
  if (lane < 8) st[lane * 8 + wid] = (f32x2){s, q};
}
template <int N> DI void ln_apply(u32x4 (&v)[N], LAS const f32x2* st, int lane, float invn) {
  float s = 0.f, q = 0.f;
#pragma unroll
  for (int i = 0; i < 4; ++i) { const f32x4 t = *(LAS const f32x4*)(st + (lane & 7) * 8 + 2 * i); s += t[0] + t[2]; q += t[1] + t[3]; }
  float mb, rs; ln_stats(s, q, invn, mb, rs);
#pragma unroll
  for (int i = 0; i < N; ++i)
#pragma unroll
    for (int e = 0; e < 4; ++e) v[i][e] = cvtpk((bflo(v[i][e]) - mb) * rs, (bfhi(v[i][e]) - mb) * rs);
}

constexpr int k3_cum(int t) { const int a = 2 * t + 4, b = t + 6; return a < b ? a : b < 32 ? b : 32; }
struct WO {
  rsrc_t rw; unsigned vo, vl, l0, so;
  DI void dma(int pc) const { dma16<0>(rw, pc >= 9 ? vl : vo, so + pc * 8192, l0 + pc * 1024); }
  FL fl; u32x2 f; unsigned voF; int wid;
  static constexpr int extra(int t) { return (t >= 2 && t < 13) + (t == 3); }
  template <int N> DI void at() { if constexpr (N % 4 == 1 && N / 4 >= 2 && N / 4 < 13) dma(N / 4 - 2); if constexpr (N == 12) fl_ld(fl, f, voF); }
  template <int T> DI void pre() { if constexpr (T == 5) { if (wid == 0) fl_chk<false>(fl, f, voF); } }
};
struct K3B { rsrc_t rA, rB; unsigned voA[2], voB[2], soA, m0; LAS const char* aR; LAS const char* bR; };
struct K3F { bf16x8 a[2], w[3]; };
template <int T, int P> DI void k3b_dma(const K3B& c) {
  if constexpr (T < 32) {
    if constexpr (P == 0) dma16<0>(c.rA, c.voA[0], c.soA + T * 128, c.m0 + ((T + 3) % 5) * 32768);
    if constexpr (P == 1) dma16n<1024>(c.rA, c.voA[1], c.soA + T * 128);
    if constexpr (P == 2) dma16<0>(c.rB, c.voB[0], T * (64 * ZLD * 2), c.m0 + ((T + 3) % 5) * 32768 + 16384);
    if constexpr (P == 3) dma16n<1024>(c.rB, c.voB[1], T * (64 * ZLD * 2));
  }
}
template <int T, int G> DI void k3b_rd(const K3B& c, K3F& f) {
  if constexpr (T < 32) {
    constexpr int o = ((T + 3) % 5) * 32768;
    if constexpr (G == 1) { f.a[0] = ldsr(c.aR + o); f.a[1] = ldsr(c.aR + o + 4096); }
    else { constexpr int j = G ? G - 1 : 0; f.w[j] = ldstr(c.bR + o + j * 64, c.bR + o + j * 64 + 8 * 192); }
  }
}
template <int T> DI void k3b_stage(const K3B& c, const K3F& f, K3F& fn, f32x16 (&o)[6]) {
  constexpr int N = k3_cum(T - 1), M = k3_cum(T) - N == 2 ? N + 1 : 32;
  if constexpr (T < 31) { WAITVL(4 * (N - T - 2) + (T == 0 ? 4 : 0)); BAR(); SB(); }
#define BM(i, j) o[3 * (i) + (j)] = MFMA(f.w[j], f.a[i], o[3 * (i) + (j)]); SB()
  BM(0, 0); k3b_rd<T + 1, 0>(c, fn); SB(); BM(1, 0); k3b_rd<T + 1, 1>(c, fn); k3b_dma<N, 0>(c); SB(); BM(0, 1); k3b_rd<T + 1, 2>(c, fn); k3b_dma<N, 1>(c); SB();
  BM(1, 1); k3b_rd<T + 1, 3>(c, fn); k3b_dma<N, 2>(c); SB(); BM(0, 2); k3b_dma<N, 3>(c); k3b_dma<M, 0>(c); k3b_dma<M, 1>(c); SB(); BM(1, 2); k3b_dma<M, 2>(c); k3b_dma<M, 3>(c); SB();
}
template <int T> DI void k3b_run(const K3B& c, K3F& f0, K3F& f1, f32x16 (&o)[6]) {
  if constexpr (T < 32) { k3b_stage<T>(c, f0, f1, o); k3b_stage<T + 1>(c, f1, f0, o); k3b_run<T + 2>(c, f0, f1, o); }
}
template <int S> DI void unit_put(LAS char* p, const f32x16& a) {
  *(LAS f32x4*)p = (f32x4){a[8 * S], a[8 * S + 1], a[8 * S + 2], a[8 * S + 3]}; *(LAS f32x4*)(p + 1024) = (f32x4){a[8 * S + 4], a[8 * S + 5], a[8 * S + 6], a[8 * S + 7]};
}
template <int KG, int J> DI void unit_send(LAS char* ex, const f32x16 (&o)[2 * J], float (&v)[J][8]) {
#pragma unroll
  for (int u = 0; u < 4; ++u)
#pragma unroll
    for (int j = 0; j < J; ++j) {
      if (u == KG) {
#pragma unroll
        for (int e = 0; e < 8; ++e) { v[j][e] = o[J * (u >> 1) + j][8 * (u & 1) + e]; asm volatile("" : "+v"(v[j][e])); }
      } else {
        LAS char* p = ex + ((u * 3 + (KG > u ? KG - 1 : KG)) * J + j) * 2048;
        if (u & 1) unit_put<1>(p, o[J * (u >> 1) + j]); else unit_put<0>(p, o[J * (u >> 1) + j]);
      }
    }
}
template <int J> DI void unit_reduce(LAS char* ex, int kg, const f32x16 (&o)[2 * J], float (&v)[J][8]) {
  if (kg == 0) unit_send<0, J>(ex, o, v); else if (kg == 1) unit_send<1, J>(ex, o, v); else if (kg == 2) unit_send<2, J>(ex, o, v); else unit_send<3, J>(ex, o, v);
  WAITL0(); BAR(); SB();
  ex += kg * (3 * J * 2048);
#pragma unroll
  for (int j = 0; j < J; ++j)
#pragma unroll
    for (int s = 0; s < 3; ++s) {
      const f32x4 a = *(LAS const f32x4*)(ex + (s * J + j) * 2048), b = *(LAS const f32x4*)(ex + (s * J + j) * 2048 + 1024);
#pragma unroll
      for (int e = 0; e < 4; ++e) { v[j][e] += a[e]; v[j][4 + e] += b[e]; }
    }
}

__global__ __launch_bounds__(512, 2) void mega(Args) {
  extern __shared__ __attribute__((aligned(16))) char lds_[];
  LAS char* lds = (LAS char*)lds_;
  const unsigned ldsb = (unsigned)(size_t)lds;
  FL fl;
  GC q; GR qr; ZE ze;
  p1_kvz(lds, fl, q, qr, ze);

  int tid = threadIdx.x;
  asm volatile("" : "+v"(tid));
  char* ws = kargs()->ws;
  const int wid = rfl(tid >> 6);
  const int bid = blockIdx.x, xcd = bid & 7, idx = bid >> 3;
  TSD; TS(0);
  K2C c;
  K2S S;
  unsigned ctxA;
  {
    const int lane = tid & 63, r = lane & 31, h = lane >> 5, q4 = (lane >> 2) & 3;
    const int head = xcd * 4 + (idx >> 3), j = idx & 7, g = wid >> 2, wq = wid & 3;
    c.rK = mkrsrc(ws + WS_K + (size_t)head * 524288, 524288); c.rV = mkrsrc(ws + WS_V + (size_t)head * 524288, 524288);
    c.rC = mkrsrc(ws + WS_C, 16u << 20);
    c.vo = lane * 16; c.ldsb = ldsb; c.mS = wid * 2048; c.mP = g * 65536 + wq * 4096;
    c.n1 = 2 * j + 2; c.g = g; c.qrow = r - 4 * h;
    const int rbA = (15 - j) * 4 + wq, rbB = g ? j * 4 + wq : rbA;
    c.rowA = rbA * 32; c.rowB = rbB * 32;
    ctxA = (rbA * 32 + head) * 8192;
    const unsigned ctxB = (rbB * 32 + head) * 8192;
    const int ch = tid >> 3, zr = bid * 8 + (lane & 7), zcg = 7 + (lane & 7);
    QX x{fl, c, ze};
    x.bid = bid; x.wid = wid; x.rz = mkrsrc(ws + WS_Z2, SEQ * FH * 2);
    x.voF = F_KV + (((idx & ~7) + (lane & 7)) * 8 + xcd) * 8; x.voZ = F_Z + ((((bid >> 4) & 3) * 8 + (zcg & 7)) * 8 + (bid >> 6) * 2 + (zcg >> 3)) * 8;
    x.kw = lds + c.mS + lane * 16;
    x.zo[0] = (zr >> 5) * 43 * 1024 + (ch >> 1) * 1024 + (ch & 1) * 512 + (zr & 31) * 16; x.zo[1] = ch < 22 ? x.zo[0] + 32768 : 0x80000000u;
    LAS f32x2* st = (LAS f32x2*)(lds + 98304);
#pragma unroll
    for (int i = 0; i < 4; ++i) { q.bR[i] = lds + 65536 + (8 * h + q4) * 256 + ((i ^ q4) * 64) + ((lane >> 4) & 1) * 32 + (lane & 3) * 8; S.o[i] = (f32x16){}; }
    TS(1);
    WAITV(11); BAR(); SB();
    fl_pub(fl, F_KV + bid * 8, tid == 0);
    TS(2);
    gc_first<8, 1024>(q, qr);
    gc_run<8, 8192, 1024, 0>(q, qr, S.o, x);
    TS(3);
#pragma unroll
    for (int i = 0; i < 4; ++i) {
      S.o[i] *= QSCALE;
      S.q[2 * i] = pack8<0>(S.o[i]); S.q[2 * i + 1] = pack8<1>(S.o[i]);
      S.o[i] = (f32x16){};
    }
    if (!g) {
#pragma unroll
      for (int i = 0; i < 8; ++i) *(LAS u32x4*)(lds + 131072 + wq * 8192 + i * 1024 + lane * 16) = S.q[i];
    }
    S.negm = (f32x16){}; S.m = 0.f; S.l = 0.f;
    f32x16 sa[2], sb[2];
    const unsigned qA = c.ldsb + 131072 + wq * 8192 + c.vo;
    S.rk = lds + lane * 16;
#pragma unroll
    for (int s = 0; s < 16; ++s) sa[s >> 3] = MFMA(k2_frag<1>(S.rk, s), __builtin_bit_cast(bf16x8, S.q[s & 7]), (s & 7) == 0 ? S.negm : sa[s >> 3]);
    asm volatile("s_waitcnt vmcnt(0)" : "+v"(x.zv[0]), "+v"(x.zv[1])::"memory");
    ln_part<2>(x.zv, st, wid, lane);
    if (63 > c.rowB) k2_mask(sa, c.rowB + c.qrow);
    k2_rescale(S, sa, k2_max(sa), true);
    SB();
    WAITL0(); BAR(); SB();
    dma16<0>(c.rK, c.vo, k2_so(c, 2), k2_m0(c, 2)); dma16n<1024>(c.rK, c.vo, k2_so(c, 2));
#pragma unroll
    for (int i = 0; i < 3; ++i) S.f[i] = k2_frag<0>(S.rk, i);
    {
      ln_apply<2>(x.zv, st, lane, 1.f / FH);
      const rsrc_t ro = mkrsrc(ws + WS_Z2N, SEQ * ZLD * 2);
      const unsigned oo = ((zr & ~12) | ((zr & 4) << 1) | ((zr & 8) >> 1)) * (ZLD * 2) + ch * 16;
      if (ch >= 22) x.zv[1] = ((u32x4){0u, 0u, 0u, 0u});
      bst16(x.zv[0], ro, oo, 0); bst16(x.zv[1], ro, ch < 32 ? oo + 1024 : 0x80000000u, 0);
    }
    unsigned pf = 0;
    TS(4);
    for (int n = 0; n < 16; n += 2) {
      k2_step<0, true>(c, S, n, sa, sb, qA, ctxB);
      if (n == 0) fl_pub(fl, F_ZN + bid * 8, wid == 0 && c.vo == 0);
      if (n == 8 && xcd >= 4 && wid == 0) {
        u32x2 fz[4];
#pragma unroll
        for (int i = 0; i < 4; ++i) fl_ld(fl, fz[i], F_ZN + i * 512 + (c.vo >> 1));
        WAITV(0);
        fl_chk<false>(fl, fz[0], F_ZN + (c.vo >> 1)); fl_chk<false>(fl, fz[1], F_ZN + 512 + (c.vo >> 1));
        fl_chk<false>(fl, fz[2], F_ZN + 1024 + (c.vo >> 1)); fl_chk<true>(fl, fz[3], F_ZN + 1536 + (c.vo >> 1));
        SB();
      }
      if (n >= TCH && n < TCH + 6) {
        const KA ka = kargs();
        const unsigned w = bid * 8 + wid;
        const rsrc_t rp = n == TCH ? mkrsrc(ka->wsp, SEQ * SEQ * 2) : n == TCH + 2 ? mkrsrc(ka->wo, 4096 * DM * 2) : mkrsrc(ka->wout, FH * DM * 2);
        asm volatile("buffer_load_dword %0, %1, %2, %3 offen" : "+v"(pf) : "v"(c.vo * 4), "s"(rp), "s"(n == TCH + 2 ? (w & 1023) * 4096 : w < 2048 - (n - TCH) * 469 ? w * 4096 : 0) : "memory");
        SB();
      }
      k2_step<1, true>(c, S, n + 1, sb, sa, qA, ctxB);
    }
    asm volatile("" ::"v"(pf));
    TS(5);
    k2_step<0, false>(c, S, 16, sa, sb, qA, ctxB);
    TS(6);
  }
  GC ca; K3B cb;
  asm volatile("" : "+v"(c.vo));
  const int lane = c.vo >> 4, r = lane & 31, h = lane >> 5, q4 = (lane >> 2) & 3;
  const int rb3 = xcd < 4 ? idx >> 2 : (xcd - 4) * 4 + (idx >> 3), wm = wid & 1, kg = wid >> 1;
  const int k16 = 6 * (idx & 7) + (kg & 1);
  const unsigned zo = ((rb3 * 4 + 2 * wm + (kg >> 1)) * 43 + k16) * 1024;
  if (xcd < 4) {
    ca.rA = c.rC; ca.rB = mkrsrc(kargs()->wo, 4096 * DM * 2);
    ca.vo = lane * 16; ca.voB[0] = (lane >> 4) * 1024 + (((lane & 15) ^ ((lane >> 4) << 2)) * 16); ca.voB[1] = ca.voB[0] + 3072;
    ca.soA = ((rb3 * 8 + wid) * 256 + xcd * 64) * 1024; ca.soB = (xcd * 1024 + wid * 8) * 1024 + (idx & 3) * 256; ca.m0 = ldsb + 98304 + wid * 2048;
    gc_dma4<16, 1024>(ca);
  } else {
    const KA ka = kargs();
    cb.rA = mkrsrc(ka->wsp, SEQ * SEQ * 2); cb.rB = mkrsrc(ws + WS_Z2N, SEQ * ZLD * 2);
#pragma unroll
    for (int i = 0; i < 2; ++i) {
      cb.voA[i] = (8 * i + (lane >> 3)) * 4096 + (((lane & 7) ^ (4 * i + (lane >> 4))) * 16) - i * 1024;
      const int o = (2 * wid + i) * 64 + lane;
      cb.voB[i] = wid < 6 ? (o / 12) * (ZLD * 2) + (o % 12) * 16 + (idx & 7) * 192 - i * 1024 : 0x80000000u;
    }
    cb.soA = (rb3 * 128 + wid * 16) * 4096; cb.m0 = ldsb + wid * 2048;
#define B4(T) k3b_dma<T, 0>(cb); k3b_dma<T, 1>(cb); k3b_dma<T, 2>(cb); k3b_dma<T, 3>(cb)
    B4(0); B4(1);
  }
  SB();
  k2_finishA(c, S, lds, wid, ctxA);
  u32x2 fc;
  const int cblk = 2 * (idx >> 2) + ((lane >> 2) & 1);
  const unsigned voC = F_CTX + (((lane & 3) * 8 + (cblk < 8 ? cblk : 15 - cblk)) * 8 + 2 * (xcd & 3)) * 128;
  fl_ld(fl, fc, voC);
  TS(7); WAITV(0); BAR(); SB();
  fl_pub(fl, F_CTX + bid * 128, wid == 0 && lane == 0);
  TS(8); TSW(2); TSR; TS(0);
  WO wo;
  wo.rw = mkrsrc(kargs()->wout, FH * DM * 2); wo.vo = (lane >> 3) * 1024 + (((lane & 7) ^ (((lane >> 4) & 1) << 2)) * 16); wo.vl = wid == 7 ? 0x80000000u : wo.vo;
  wo.l0 = ldsb + wid * 11264; wo.so = wid * 11 * 8192 + (idx >> 2) * 128;
  if (xcd < 4) {
    GR g;
    if (wid == 0) fl_chk<false>(fl, fc, voC);
    BAR(); SB();
    TS(1);
    gc_lda2<16>(ca, g);
    wo.fl = fl; wo.wid = wid; wo.voF = voC + 128;
#pragma unroll
    for (int i = 0; i < 4; ++i) ca.bR[i] = lds + 98304 + (4 * h + q4) * 256 + ((i ^ q4) * 64) + ((lane >> 4) & 1) * 32 + (lane & 3) * 8;
    f32x16 o[4] = {};
    WAITV(4); SB();
    TS(2);
    gc_first<16, 2048>(ca, g);
    gc_run<16, 1024, 2048, 0>(ca, g, o, wo);
    TS(3);
    const rsrc_t rp = mkrsrc(ws + WS_P, 8u << 20);
    const unsigned so = ((xcd * 64 + rb3 * 8 + wid) * 16 + (idx & 3) * 4) * 2048;
#pragma unroll
    for (int i = 0; i < 4; ++i) { bst16(pack8<0>(o[i]), rp, ca.vo, so + i * 2048); bst16(pack8<1>(o[i]), rp, ca.vo, so + i * 2048 + 1024); }
  } else {
    u32x4 z[3]; float b;
    const rsrc_t rz = mkrsrc(ws + WS_Z1, SEQ * FH * 2);
#pragma unroll
    for (int j = 0; j < 3; ++j) asm volatile("buffer_load_dwordx4 %0, %1, %2, %3 offen sc1" : "=v"(z[j]) : "v"(lane * 16), "s"(rz), "s"(zo + j * 2048) : "memory");
    asm volatile("buffer_load_ushort %0, %1, %2, %3 offen" : "=v"(b) : "v"(r * 2), "s"(mkrsrc(kargs()->bsp, SEQ * 2)), "s"((rb3 * 4 + 2 * wm + (kg >> 1)) * 64) : "memory");
    cb.aR = lds + (wm * 64 + r) * 128 + ((h ^ ((r >> 1) & 7) ^ (2 * kg)) * 16);
    cb.bR = lds + 16384 + (16 * kg + 4 * h + q4) * 192 + ((lane >> 4) & 1) * 32 + (lane & 3) * 8;
    f32x16 o[6] = {};
    K3F f0, f1;
    k3b_rd<0, 0>(cb, f0); k3b_rd<0, 1>(cb, f0); k3b_rd<0, 2>(cb, f0); k3b_rd<0, 3>(cb, f0); SB();
    k3b_run<0>(cb, f0, f1, o);
    TS(3);
    BAR();
    asm volatile("" : "+v"(z[0]), "+v"(z[1]), "+v"(z[2]), "+v"(b));
    b = __uint_as_float(__float_as_uint(b) << 16);
    float v[3][8];
    unit_reduce<3>(lds + wm * 73728 + lane * 16, kg, o, v);
    const rsrc_t rh = mkrsrc(ws + WS_H, SEQ * FH * 2);
#pragma unroll
    for (int j = 0; j < 3; ++j) {
      u32x4 w;
#pragma unroll
      for (int e = 0; e < 4; ++e) w[e] = cvtpk(bflo(z[j][e]) * (v[j][2 * e] + b), bfhi(z[j][e]) * (v[j][2 * e + 1] + b));
      if (k16 + 2 * j < 43) bst16(w, rh, lane * 16, zo + j * 2048);
    }
    fl_pub(fl, 0, bid == 7 && wid == 0 && lane == 0);
  }
  {
    const int rb = xcd * 4 + (idx & 3), cb = idx >> 2, rb32 = rb * 2 + wm;
    u32x4 hf[11], pp[4]; u32x2 xv[2], fh;
    const unsigned voH = F_P + ((((rb >> 1) & 3) * 8 + (lane & 7)) * 8 + 4 + (rb >> 3)) * 128, voP = F_P + ((xcd * 4 + (cb >> 1)) * 8 + (lane & 3)) * 128;
    fl_ld(fl, fh, voH);
    TS(4); WAITV(0); BAR(); SB();
    fl_pub(fl, F_P + bid * 128, wid == 0 && lane == 0);
    TS(5); TSW(3); TSR; TS(0);
    const KA ka = kargs();
    if (xcd >= 4) {
#pragma unroll
      for (int pc = 0; pc < 11; ++pc) wo.dma(pc);
    }
    if (wid == 7) fl_chk<false>(fl, fh, voH);
    BAR(); SB();
    TS(1);
    {
      const rsrc_t rh = mkrsrc(ws + WS_H, SEQ * FH * 2), rx = mkrsrc(ka->x, SEQ * DM * 2);
#pragma unroll
      for (int i = 0; i < 11; ++i) ld16(hf[i], rh, i == 10 && kg == 3 ? 0x80000000u : lane * 16, (rb32 * 43 + kg * 11 + i) * 1024);
      fl_ld(fl, fh, voP);
#pragma unroll
      for (int i = 0; i < 2; ++i)
        asm volatile("buffer_load_dwordx2 %0, %1, %2, %3 offen offset:%4" : "=v"(xv[i]) : "v"(r * 1024 + h * 8), "s"(rx), "s"(rb32 * 32768 + cb * 128 + kg * 32), "i"(i * 16) : "memory");
    }
    LAS const char* bR[2];
#pragma unroll
    for (int j = 0; j < 2; ++j) bR[j] = lds + kg * 22528 + (4 * h + q4) * 128 + ((j ^ (q4 >> 1)) * 64) + ((lane >> 4) & 1) * 32 + (lane & 3) * 8;
    f32x16 o[2] = {};
    WAITV(3); BAR(); SB();
    TS(2);
#pragma unroll
    for (int i = 0; i < 11; ++i) asm volatile("" : "+v"(hf[i]));
#pragma unroll
    for (int ks = 0; ks < 11; ++ks)
#pragma unroll
      for (int j = 0; j < 2; ++j) o[j] = MFMA(ldstr(bR[j] + ks * 2048, bR[j] + ks * 2048 + 1024), __builtin_bit_cast(bf16x8, hf[ks]), o[j]);
    TS(3);
    WAITVL(0);
    if (wid == 7) fl_chk<false>(fl, fh, voP);
    BAR(); SB();
    const rsrc_t rp = mkrsrc(ws + WS_P, 8u << 20);
#pragma unroll
    for (int i = 0; i < 4; ++i) ld16(pp[i], rp, lane * 16, ((i * 64 + rb32) * 32 + cb * 4 + kg) * 1024);
    float v[1][8];
    unit_reduce<1>(lds + wm * 24576 + lane * 16, kg, o, v);
    asm volatile("s_waitcnt vmcnt(0)" : "+v"(pp[0]), "+v"(pp[1]), "+v"(pp[2]), "+v"(pp[3]), "+v"(xv[0]), "+v"(xv[1])::"memory");
    u32x4 w;
#pragma unroll
    for (int e = 0; e < 4; ++e)
      w[e] = cvtpk(v[0][2 * e] + bflo(xv[e >> 1][e & 1]) + ((bflo(pp[0][e]) + bflo(pp[1][e])) + (bflo(pp[2][e]) + bflo(pp[3][e]))),
                   v[0][2 * e + 1] + bfhi(xv[e >> 1][e & 1]) + ((bfhi(pp[0][e]) + bfhi(pp[1][e])) + (bfhi(pp[2][e]) + bfhi(pp[3][e]))));
    bst16<0>(natural8(w), mkrsrc(ka->out, SEQ * DM * 2), r * 1024 + h * 16, rb32 * 32768 + cb * 128 + kg * 32);
    TS(4); TSW(4);
  }
}

extern "C" void kernel_launch(void* const* d_in, const int* in_sizes, int n_in, void* d_out, int out_size, void* d_ws, size_t ws_size, hipStream_t stream) {
  (void)in_sizes; (void)n_in; (void)out_size; (void)ws_size;
  const Args a = {(const u16*)d_in[0], (const u16*)d_in[1], (const u16*)d_in[2], (const u16*)d_in[3], (const u16*)d_in[4], (const u16*)d_in[5],
                  (const u16*)d_in[7], (const u16*)d_in[8], (const u16*)d_in[9], (const u16*)d_in[10], (char*)d_ws, (u16*)d_out};
  (void)hipFuncSetAttribute((const void*)mega, hipFuncAttributeMaxDynamicSharedMemorySize, LDSZ);
  mega<<<256, 512, LDSZ, stream>>>(a);
}
```

```cpp
#include <hip/hip_runtime.h>
#include <hip/hip_bf16.h>
#include <stdint.h>

typedef __bf16 bf16x8 __attribute__((ext_vector_type(8)));
typedef __bf16 bf16x4 __attribute__((ext_vector_type(4)));
typedef __bf16 bf16x2 __attribute__((ext_vector_type(2)));
typedef float f32x16 __attribute__((ext_vector_type(16)));
typedef float f32x4 __attribute__((ext_vector_type(4)));
typedef float f32x2 __attribute__((ext_vector_type(2)));
typedef unsigned u32x4 __attribute__((ext_vector_type(4)));
typedef unsigned u32x2 __attribute__((ext_vector_type(2)));
typedef unsigned short u16;
typedef __amdgpu_buffer_rsrc_t rsrc_t;
#define LAS __attribute__((address_space(3)))
#define DI __device__ __forceinline__
#define SB() __builtin_amdgcn_sched_barrier(0)
#define MFMA(a, b, c) __builtin_amdgcn_mfma_f32_32x32x16_bf16((a), (b), (c), 0, 0, 0)
#define WAITV(n) asm volatile("s_waitcnt vmcnt(" #n ")" ::: "memory")
#define WAITL0() asm volatile("s_waitcnt lgkmcnt(0)" ::: "memory")
#define BAR() do { asm volatile("" ::: "memory"); __builtin_amdgcn_s_barrier(); asm volatile("" ::: "memory"); } while (0)

#define TSD
#define TSR
#define TS(i)
#define TSW(k)
constexpr int SEQ = 2048, DM = 512, NH = 32, HD = 128, FH = 688, FI = 1376, ZLD = 768;
constexpr size_t WS_XN = 0;
constexpr size_t WS_Z1 = WS_XN + (size_t)SEQ * DM * 2;
constexpr size_t WS_Z2 = WS_Z1 + (size_t)SEQ * FH * 2;
constexpr size_t WS_Z2N = WS_Z2 + (size_t)SEQ * FH * 2;
constexpr size_t WS_H = WS_Z2N + (size_t)SEQ * ZLD * 2;
constexpr size_t WS_K = 48u << 20;
constexpr size_t WS_V = WS_K + (16u << 20);
constexpr size_t WS_C = WS_V + (16u << 20);
constexpr size_t WS_P = WS_C + (16u << 20);
constexpr size_t WS_F = 120u << 20;
static_assert(WS_H + (size_t)SEQ * FH * 2 <= WS_K, "ws map");

DI unsigned rfl(unsigned v) { return __builtin_amdgcn_readfirstlane(v); }
DI unsigned cvtpk(float lo, float hi) { f32x2 v = {lo, hi}; bf16x2 b = __builtin_convertvector(v, bf16x2); return __builtin_bit_cast(unsigned, b); }
DI float bflo(unsigned u) { return __uint_as_float(u << 16); }
DI float bfhi(unsigned u) { return __uint_as_float(u & 0xffff0000u); }
DI float rbf(float v) { return bflo(cvtpk(v, v) & 0xffffu); }
template <int S> DI u32x4 pack8(const f32x16& c) {
  u32x4 w = {cvtpk(c[8 * S + 0], c[8 * S + 1]), cvtpk(c[8 * S + 2], c[8 * S + 3]), cvtpk(c[8 * S + 4], c[8 * S + 5]), cvtpk(c[8 * S + 6], c[8 * S + 7])};
  return w;
}
DI rsrc_t mkrsrc(const void* p, unsigned bytes) { return __builtin_amdgcn_make_buffer_rsrc((void*)p, (short)0, (int)bytes, 0x00020000); }
template <int IMM> DI void dma16(rsrc_t r, unsigned voff, unsigned soff, unsigned m0v) {
  asm volatile("s_mov_b32 m0, %0\n\ts_nop 0\n\tbuffer_load_dwordx4 %1, %2, %3 offen offset:%4 lds" ::"s"(m0v), "v"(voff), "s"(r), "s"(soff), "i"(IMM) : "memory");
}
DI bf16x8 ldsr(LAS const char* p) { return *(LAS const bf16x8*)p; }
DI bf16x8 ldstr(LAS const char* p0, LAS const char* p1) {
  bf16x4 a = __builtin_amdgcn_ds_read_tr16_b64_v4bf16((LAS bf16x4*)p0);
  bf16x4 b = __builtin_amdgcn_ds_read_tr16_b64_v4bf16((LAS bf16x4*)p1);
  return __builtin_shufflevector(a, b, 0, 1, 2, 3, 4, 5, 6, 7);
}
DI float swap32_add(float v) { auto r = __builtin_amdgcn_permlane32_swap(__float_as_uint(v), __float_as_uint(v), false, false); return __uint_as_float(r[0]) + __uint_as_float(r[1]); }
DI float swap32_max(float v) { auto r = __builtin_amdgcn_permlane32_swap(__float_as_uint(v), __float_as_uint(v), false, false); return fmaxf(__uint_as_float(r[0]), __uint_as_float(r[1])); }
DI u32x4 natural8(u32x4 w) {
  const auto a = __builtin_amdgcn_permlane32_swap(w[0], w[2], false, false), b = __builtin_amdgcn_permlane32_swap(w[1], w[3], false, false);
  return (u32x4){a[0], b[0], a[1], b[1]};
}
template <int IMM> DI void dma16n(rsrc_t r, unsigned voff, unsigned soff) {
  asm volatile("buffer_load_dwordx4 %0, %1, %2 offen offset:%3 lds" ::"v"(voff), "s"(r), "s"(soff), "i"(IMM) : "memory");
}
template <int AUX = 16> DI void bst16(u32x4 v, rsrc_t r, unsigned vo, unsigned so) { __builtin_amdgcn_raw_buffer_store_b128(v, r, vo, so, AUX); }
#define WAITVL(n) asm volatile("s_waitcnt vmcnt(%0) lgkmcnt(0)" ::"i"(n) : "memory")
DI void ln_stats(float s, float q, float invn, float& mb, float& rs) {
  const float mean = s * invn, var = fmaxf(fmaf(-mean, mean, q * invn), 0.f);
  mb = rbf(mean); rs = 1.f / rbf(sqrtf(rbf(rbf(var) + 1e-5f)));
}
DI float dot2(unsigned a, unsigned b, float c) { return __builtin_amdgcn_fdot2_f32_bf16(__builtin_bit_cast(bf16x2, a), __builtin_bit_cast(bf16x2, b), c, false); }
enum { F_XN = 4096, F_KV = 8192, F_ZN = 12288, F_Z = 16384, F_CTX = 65536, F_P = 131072 };
struct FL { rsrc_t r; unsigned lo, hi; };
struct Args { const u16 *x, *ckv, *wq, *wk, *wv, *wo, *w_in, *wsp, *bsp, *wout; char* ws; u16* out; };
typedef __attribute__((address_space(4))) const Args* KA;
DI KA kargs() { KA a = (KA)__builtin_amdgcn_kernarg_segment_ptr(); asm volatile("" : "+s"(a)); return a; }
DI void fl_pub(const FL& f, unsigned off, bool lead) { if (lead) __builtin_amdgcn_raw_buffer_store_b64((u32x2){f.lo, f.hi}, f.r, 0, off, 16); }
DI void fl_ld(const FL& f, u32x2& v, unsigned vo) { asm volatile("buffer_load_dwordx2 %0, %1, %2, 0 offen sc1" : "=v"(v) : "v"(vo), "s"(f.r) : "memory"); }
template <bool ACQ> DI void fl_chk(const FL& f, u32x2 v, unsigned vo) {
  asm volatile("" : "+v"(v));
#pragma unroll 1
  for (int i = 0; i < (1 << 18) && __any(v[0] != f.lo || v[1] != f.hi); ++i) {
    __builtin_amdgcn_s_sleep(2); fl_ld(f, v, vo);
    asm volatile("s_waitcnt vmcnt(0)" : "+v"(v)::"memory");
  }
  if (ACQ) asm volatile("buffer_inv sc1" ::: "memory");
}
struct GC { rsrc_t rA, rB; unsigned vo, voB[2], soA, soB, m0; LAS const char* bR[4]; };
struct GR { u32x4 a[3][4]; bf16x8 w[2][4]; };
template <int NP, int LDB, int T, int P> DI void gc_dma(const GC& c) {
  if constexpr (T < NP) { if constexpr (P == 0) dma16<0>(c.rB, c.voB[0], c.soB + T * 64 * LDB, c.m0 + (T & 3) * 16384); else dma16n<1024>(c.rB, c.voB[1], c.soB + T * 64 * LDB); }
}
template <int NP, int T, int KS> DI void gc_lda(const GC& c, GR& g) {
  if constexpr (T < NP) asm volatile("buffer_load_dwordx4 %0, %1, %2, %3 offen offset:%4 sc1" : "=v"(g.a[T % 3][KS]) : "v"(c.vo), "s"(c.rA), "s"(c.soA + T * 4096), "i"(KS * 1024) : "memory");
}
template <int NP, int HO, int N, int J> DI void gc_rd(const GC& c, GR& g) {
  if constexpr (N < 4 * NP) { LAS const char* b = c.bR[J] + ((N >> 2) & 3) * 16384 + (N & 3) * 4096; g.w[N & 1][J] = ldstr(b, b + HO); }
}
template <int NP, int LDB, int HO, int N, class X> DI void gc_step(const GC& c, GR& g, f32x16 (&o)[4], X& x) {
  constexpr int T = N >> 2, KS = N & 3;
  if constexpr (KS == 3 && T + 1 < NP) {
    x.template pre<T>();
    WAITVL((T > 0 && T + 3 < NP ? 2 : 0) + (T + 2 < NP ? 4 : 0) + X::extra(T)); BAR();
    asm volatile("" : "+v"(g.a[(T + 1) % 3][0]), "+v"(g.a[(T + 1) % 3][1]), "+v"(g.a[(T + 1) % 3][2]), "+v"(g.a[(T + 1) % 3][3])); SB();
  }
  const bf16x8 a = __builtin_bit_cast(bf16x8, g.a[T % 3][KS]);
  o[0] = MFMA(g.w[N & 1][0], a, o[0]); SB(); gc_rd<NP, HO, N + 1, 0>(c, g); SB();
  o[1] = MFMA(g.w[N & 1][1], a, o[1]); SB(); gc_rd<NP, HO, N + 1, 1>(c, g);
  if constexpr (KS == 3) gc_dma<NP, LDB, T + 4, 0>(c); else gc_lda<NP, T + 2, KS>(c, g);
  SB();
  o[2] = MFMA(g.w[N & 1][2], a, o[2]); SB(); gc_rd<NP, HO, N + 1, 2>(c, g); x.template at<N>(); SB();
  o[3] = MFMA(g.w[N & 1][3], a, o[3]); SB(); gc_rd<NP, HO, N + 1, 3>(c, g);
  if constexpr (KS == 3) gc_dma<NP, LDB, T + 4, 1>(c); else if constexpr (KS == 2) gc_lda<NP, T + 2, 3>(c, g);
  SB();
}
template <int NP, int LDB, int HO, int N, class X> DI void gc_run(const GC& c, GR& g, f32x16 (&o)[4], X& x) {
  if constexpr (N < 4 * NP) { gc_step<NP, LDB, HO, N>(c, g, o, x); gc_run<NP, LDB, HO, N + 1>(c, g, o, x); }
}
template <int NP, int LDB> DI void gc_dma4(const GC& c) {
  gc_dma<NP, LDB, 0, 0>(c); gc_dma<NP, LDB, 0, 1>(c); gc_dma<NP, LDB, 1, 0>(c); gc_dma<NP, LDB, 1, 1>(c);
  gc_dma<NP, LDB, 2, 0>(c); gc_dma<NP, LDB, 2, 1>(c); gc_dma<NP, LDB, 3, 0>(c); gc_dma<NP, LDB, 3, 1>(c);
}
template <int NP> DI void gc_lda2(const GC& c, GR& g) {
  gc_lda<NP, 0, 0>(c, g); gc_lda<NP, 0, 1>(c, g); gc_lda<NP, 0, 2>(c, g); gc_lda<NP, 0, 3>(c, g);
  gc_lda<NP, 1, 0>(c, g); gc_lda<NP, 1, 1>(c, g); gc_lda<NP, 1, 2>(c, g); gc_lda<NP, 1, 3>(c, g);
}
template <int NP, int HO> DI void gc_first(const GC& c, GR& g) {
  asm volatile("" : "+v"(g.a[0][0]), "+v"(g.a[0][1]), "+v"(g.a[0][2]), "+v"(g.a[0][3]));
  gc_rd<NP, HO, 0, 0>(c, g); gc_rd<NP, HO, 0, 1>(c, g); gc_rd<NP, HO, 0, 2>(c, g); gc_rd<NP, HO, 0, 3>(c, g); SB();
}

constexpr int LDSZ = 163840;
constexpr float QSCALE = 0.08838834764831845f * 1.4426950408889634f;
struct K1C {
  rsrc_t rA, rW0, rW1, rX, rI, rK;
  unsigned voA[2], voB, voI[2], vo, m0A, m0B, soA, soB, soX, soK; int rot;
  LAS const char* aR[4]; LAS const char* bR;
  rsrc_t rP, rN; unsigned voP, voT, soP, voN, soN, voF; u32x4 xn; FL fl;
};
constexpr int NPRO = 8, QUO = 4;
struct K1Op { int kind, item, piece; };
constexpr int k1_xslot(int i) { return i % 5 == 0 ? 6 : i % 5 == 1 ? 7 : i == 2 ? 4 : i == 3 ? 5 : i == 4 ? 9 : 4; }
constexpr int k1_wslot(int i) { return i % 5 == 0 ? 2 : i % 5 == 1 ? 3 : i == 3 ? 0 : i == 4 ? 1 : 8; }
constexpr K1Op k1_free(int p, int i) {
  if (p < 0) { const int q = p + 10; constexpr int it[10] = {0, 0, 1, 1, 2, 3, 2, 4, 5, 3}; return i < 2 ? K1Op{q % 3 == 0 ? 2 : 1, it[q], i} : K1Op{0, 0, 0}; }
  if (i < 2) return p < 26 ? K1Op{1, p + 6, i} : p == 26 ? K1Op{4, 0, i} : p == 27 ? K1Op{4, 1, i} : p == 28 ? K1Op{3, 2, i} : p == 29 ? K1Op{3, 3, i} : p == 30 ? K1Op{4, 3, i}
                  : p == 31 ? K1Op{4, 4, i} : p < 35 ? K1Op{3, p - 27, i} : K1Op{0, 0, 0};
  if (i == 2) return p == 0 ? K1Op{0, 0, 0} : p < 25 ? K1Op{2, (p - 1) / 2 + 4, (p - 1) & 1} : p < 29 ? K1Op{3, (p - 25) / 2, (p - 1) & 1} : p < 31 ? K1Op{4, 2, (p - 1) & 1}
                   : p == 31 ? K1Op{3, 4, 0} : p < 35 ? K1Op{4, p - 27, 0} : K1Op{0, 0, 0};
  return p == 31 ? K1Op{3, 4, 1} : p >= 32 && p < 35 ? K1Op{4, p - 27, 1} : K1Op{0, 0, 0};
}
constexpr int k1_cum(int p) {
  int n = 0;
  for (int q = -10; q <= p; ++q) for (int i = 0; i < 4; ++i) n += k1_free(q, i).kind != 0;
  return p < -1 ? 0 : n < NPRO + QUO * (p + 1) ? n : NPRO + QUO * (p + 1);
}
constexpr K1Op k1_seq(int n) {
  for (int q = -10; q < 40; ++q) for (int i = 0; i < 4; ++i) if (k1_free(q, i).kind && n-- == 0) return k1_free(q, i);
  return K1Op{0, 0, 0};
}
constexpr K1Op k1_op(int p, int i) {
  if (p < 0 || i < 4) return k1_cum(p - 1) + i < k1_cum(p) ? k1_seq(k1_cum(p - 1) + i) : K1Op{0, 0, 0};
  if (i == 4) return p == 0 ? K1Op{7, 0, 0} : p >= 16 && p < 31 && !(p & 1) ? K1Op{5, 0, (p - 16) / 2} : p == 32 || p == 33 ? K1Op{5, 1, p - 32} : p >= 34 && p < 37 ? K1Op{5, 1, 2 * p - 66} : K1Op{0, 0, 0};
  return p >= 34 && p < 37 ? K1Op{5, 1, 2 * p - 65} : p == 17 ? K1Op{8, 0, 0} : p == 37 ? K1Op{6, 0, 0} : p == 9 ? K1Op{6, 1, 0} : K1Op{0, 0, 0};
}
constexpr bool k1_needs(int p, K1Op o) {
  return p < 32 ? (o.kind == 1 && o.item == p) || (o.kind == 2 && o.item == p / 2) : (o.kind == 3 || o.kind == 4) && o.item == p - 32;
}
constexpr int k1_wait(int p, int upto) {
  int n = 0, got = 0;
  for (int q = -1; q < upto; ++q) for (int i = 0; i < (q < 0 ? 20 : 6); ++i) { const K1Op o = k1_op(q, i); if (k1_needs(p, o)) { n = 0; ++got; } else if (o.kind) ++n; }
  return got == 4 ? n : -1;
}
struct K1P { unsigned t; u32x2 f; };
struct K1A { f32x16 k[2], v[2]; };
template <int P, int I> DI void k1_vm(const K1C& c, const K1A& a0, const K1A& a1, K1P& pf) {
  constexpr K1Op o = k1_op(P, I);
  if constexpr (o.kind == 1) dma16<0>(o.piece ? c.rW1 : c.rW0, c.voB, c.soB + ((o.item + 2 * c.rot) & 15) * 262144, c.m0B + (o.item % 6) * 16384 + o.piece * 8192);
  if constexpr (o.kind == 2) dma16<0>(c.rA, c.voA[o.piece], c.soA + (o.item >> 3) * 131072 + ((o.item + c.rot) & 7) * 128, c.m0A + (6 + (o.item & 3)) * 16384 + o.piece * 1024);
  if constexpr (o.kind == 3) dma16<0>(c.rX, c.voA[o.piece], c.soX + ((o.item + c.rot) & 7) * 128, c.m0A + k1_xslot(o.item) * 16384 + o.piece * 1024);
  if constexpr (o.kind == 4) dma16<0>(c.rI, c.voI[o.piece], ((o.item + c.rot) & 7) * (64 * FI * 2), c.m0A + k1_wslot(o.item) * 16384 + o.piece * 1024);
  if constexpr (o.kind == 5) {
    const K1A& a = o.item ? a1 : a0;
    const f32x16& t = o.piece & 2 ? a.v[o.piece >> 2] : a.k[o.piece >> 2];
    bst16(pack8<o.piece & 1>(t), c.rK, c.vo, c.soK + ((o.piece >> 1) & 1) * (16u << 20) + o.item * 32768 + (o.piece >> 2) * 8192 + (o.piece & 1) * 1024);
  }
  if constexpr (o.kind == 7) bst16(c.xn, c.rN, c.voN, c.soN);
  if constexpr (o.kind == 8) fl_ld(c.fl, pf.f, c.voF);
  if constexpr (o.kind == 6) asm volatile("buffer_load_dword %0, %1, %2, %3 offen" : "+v"(pf.t) : "v"(o.item ? c.voT : c.voP), "s"(o.item ? c.rI : c.rP), "s"(o.item ? 0u : c.soP) : "memory");
}
struct K1F { bf16x8 a[2][2], w[2][2]; };
template <int P, int G> DI void k1_rd(const K1C& c, K1F& f) {
  if constexpr (P < 32) {
    constexpr int KS = G / 3;
    if constexpr (G % 3 == 1) { LAS const char* a = c.aR[2 * (P & 1) + KS] + ((P >> 1) & 3) * 16384; f.a[KS][0] = ldsr(a); f.a[KS][1] = ldsr(a + 4096); }
    else { LAS const char* b = c.bR + (P % 6) * 16384 + KS * 4096 + (G % 3 / 2) * 8192; f.w[KS][G % 3 / 2] = ldstr(b, b + 1024); }
  }
}
struct K1Z { bf16x8 x[2], w[2][3], d[2]; };
struct K1ZA { LAS const char* xR[2]; LAS const char* wR; LAS const char* dR; f32x16 acc[3]; float s, q, cs; };
template <int S, int K, int G> DI void k1z_rd(const K1ZA& z, K1Z& f) {
  if constexpr (S < 8) {
    constexpr int o = k1_wslot(S) * 16384 + K * 16 * 192;
    f.w[K][G] = ldstr(z.wR + o + G * 64, z.wR + o + G * 64 + 768);
    if constexpr (G == 0) f.x[K] = ldsr(z.xR[K] + k1_xslot(S) * 16384);
    if constexpr (G == 1) f.d[K] = ldstr(z.dR + o, z.dR + o + 768);
  }
}
template <int P> DI void k1_per(const K1C& c, const K1F& f, K1F& fn, K1A& a0, K1A& a1, K1P& pf, const K1ZA& z, K1Z& g) {
  K1A& a = P < 16 ? a0 : a1;
  static_assert(k1_wait(P + 1, P) >= 0, "operand not requested");
  WAITVL(k1_wait(P + 1, P)); BAR(); SB();
#define KM(ks, i) a.k[i] = MFMA(f.w[ks][0], f.a[ks][i], a.k[i]); SB()
#define VM(ks, i) a.v[i] = MFMA(f.a[ks][i], f.w[ks][1], a.v[i]); SB()
#define KR(G) if constexpr (P < 31) k1_rd<P + 1, G>(c, fn); else k1z_rd<0, (G) / 3, (G) % 3>(z, g)
  if constexpr (P == 8) fl_pub(c.fl, F_XN + blockIdx.x * 8, c.soB < 32768 && c.vo == 0);
  KM(0, 0); KR(0); SB(); KM(0, 1); KR(1); SB(); VM(0, 0); KR(2); SB();
  VM(0, 1); KR(3); k1_vm<P, 0>(c, a0, a1, pf); SB(); KM(1, 0); KR(4); k1_vm<P, 1>(c, a0, a1, pf); SB(); KM(1, 1); KR(5); k1_vm<P, 2>(c, a0, a1, pf); SB();
  VM(1, 0); k1_vm<P, 3>(c, a0, a1, pf); SB(); VM(1, 1); k1_vm<P, 4>(c, a0, a1, pf); k1_vm<P, 5>(c, a0, a1, pf);
  if constexpr (P == 24) { if (c.soB < 32768) fl_chk<false>(c.fl, pf.f, c.voF); }
  SB();
}
template <int P, int E> DI void k1_run(const K1C& c, K1F& f0, K1F& f1, K1A& a0, K1A& a1, K1P& pf, const K1ZA& z, K1Z& g) {
  if constexpr (P < E) { k1_per<P>(c, f0, f1, a0, a1, pf, z, g); k1_per<P + 1>(c, f1, f0, a0, a1, pf, z, g); k1_run<P + 2, E>(c, f0, f1, a0, a1, pf, z, g); }
}
template <int S, int K> DI void k1z_half(const K1C& c, const K1A& a1, K1ZA& z, const K1Z& f, K1Z& fn, K1P& pf) {
  const u32x4 x = __builtin_bit_cast(u32x4, f.x[K]), d = __builtin_bit_cast(u32x4, f.d[K]);
  z.acc[0] = MFMA(f.w[K][0], f.x[K], z.acc[0]); SB(); k1z_rd<S + 1, K, 0>(z, fn);
#pragma unroll
  for (int e = 0; e < 4; ++e) z.s = dot2(x[e], 0x3f803f80u, z.s);
  k1_vm<32 + S, 3 * K>(c, a1, a1, pf); SB();
  z.acc[1] = MFMA(f.w[K][1], f.x[K], z.acc[1]); SB(); k1z_rd<S + 1, K, 1>(z, fn);
#pragma unroll
  for (int e = 0; e < 4; ++e) z.q = dot2(x[e], x[e], z.q);
  k1_vm<32 + S, 3 * K + 1>(c, a1, a1, pf); SB();
  z.acc[2] = MFMA(f.w[K][2], f.x[K], z.acc[2]); SB(); k1z_rd<S + 1, K, 2>(z, fn);
#pragma unroll
  for (int e = 0; e < 4; ++e) z.cs = dot2(d[e], 0x3f803f80u, z.cs);
  k1_vm<32 + S, 3 * K + 2>(c, a1, a1, pf); SB();
}
template <int S> DI void k1z_stage(const K1C& c, const K1A& a1, K1ZA& z, const K1Z& f, K1Z& fn, K1P& pf) {
  if constexpr (S < 7) { WAITVL(k1_wait(33 + S, 32 + S)); BAR(); SB(); }
  k1z_half<S, 0>(c, a1, z, f, fn, pf); k1z_half<S, 1>(c, a1, z, f, fn, pf);
}
template <int KH> DI void k1z_send(LAS f32x4* ex, const f32x16 (&acc)[3], f32x4 (&own)[3][2]) {
#pragma unroll
  for (int nb = 0; nb < 3; ++nb)
#pragma unroll
    for (int e = 0; e < 2; ++e) {
      constexpr int o = 8 * KH, t = 8 - o;
      ex[(nb * 2 + e) * 64] = (f32x4){acc[nb][t + 4 * e], acc[nb][t + 4 * e + 1], acc[nb][t + 4 * e + 2], acc[nb][t + 4 * e + 3]};
      own[nb][e] = (f32x4){acc[nb][o + 4 * e], acc[nb][o + 4 * e + 1], acc[nb][o + 4 * e + 2], acc[nb][o + 4 * e + 3]};
      asm volatile("" : "+v"(own[nb][e]));
    }
}
struct ZE {
  LAS const f32x4* ex; LAS const float* cs; f32x4 own[3][2]; float rs, mrs; rsrc_t rz; unsigned vo; int k16, rb32;
  template <int NB> DI void gelu() const {
    u32x4 w;
#pragma unroll
    for (int e = 0; e < 2; ++e) {
      const f32x4 c0 = *(LAS const f32x4*)(cs + NB * 32 + 8 * e), c1 = *(LAS const f32x4*)(cs + 128 + NB * 32 + 8 * e);
      const f32x4 t = (ex[(NB * 2 + e) * 64] + own[NB][e]) * rs + (c0 + c1) * mrs;
      const f32x4 u = t * (t * t * -0.1029432397f - 2.302208198f);
      f32x4 d;
#pragma unroll
      for (int cc = 0; cc < 4; ++cc) d[cc] = __builtin_amdgcn_exp2f(u[cc]);
      d += 1.f;
#pragma unroll
      for (int cc = 0; cc < 4; ++cc) d[cc] = __builtin_amdgcn_rcpf(d[cc]);
      d *= t;
      w[2 * e] = cvtpk(d[0], d[1]); w[2 * e + 1] = cvtpk(d[2], d[3]);
    }
    const int k = k16 + 2 * NB;
    if (k < 43) bst16(w, rz, vo, (rb32 * 43 + k) * 1024);
    else bst16(natural8(w), rz, k < 86 ? vo : 0x80000000u, SEQ * FH * 2 + (rb32 * 43 + k - 43) * 1024);
  }
};
DI void p1_kvz(LAS char* lds, FL& fl, GC& q, GR& qr, ZE& ze) {
  const KA ka = kargs();
  char* ws = ka->ws;
  const int tid = threadIdx.x, lane = tid & 63, wid = rfl(tid >> 6), r = lane & 31, h = lane >> 5;
  const int bid = blockIdx.x, xcd = bid & 7, idx = bid >> 3, head = xcd * 4 + (idx >> 3), rb = idx & 7;
  const int wm = wid >> 2, wn = wid & 3, q4 = (lane >> 2) & 3, tt = h ^ ((r >> 1) & 7);
  const int zrb = (xcd >> 1) * 4 + (idx >> 3), cg = (xcd & 1) * 8 + rb;
  TSD; TS(0);
  K1C c;
  K1A a0 = {}, a1 = {};
  K1P pf = {};
  const unsigned ldsb = (unsigned)(size_t)lds;
  u32x4 xv;
  c.rX = mkrsrc(ka->x, SEQ * DM * 2);
  asm volatile("buffer_load_dwordx4 %0, %1, %2, %3 offen" : "=v"(xv) : "v"(lane * 16), "s"(c.rX), "s"((bid * 8 + wid) * 1024) : "memory");
  c.rA = mkrsrc(ka->ckv, SEQ * DM * 2); c.rW0 = mkrsrc(ka->wk, DM * 4096 * 2); c.rW1 = mkrsrc(ka->wv, DM * 4096 * 2);
#pragma unroll
  for (int p = 0; p < 2; ++p) c.voA[p] = (8 * p + (lane >> 3)) * 1024 + (((lane & 7) ^ (4 * p + (lane >> 4))) * 16);
  c.voB = (lane >> 4) * 8192 + (((lane & 15) ^ ((lane >> 4) << 2)) * 16);
  c.soA = (rb * 256 + wid * 16) * 1024; c.soB = wid * 4 * 8192 + head * 256;
  c.rot = (rb + 2 * (idx >> 3)) & 7;
  c.m0A = ldsb + wid * 2048; c.m0B = ldsb + wid * 1024;
#define P2(q) k1_vm<-1, q>(c, a0, a1, pf); k1_vm<-1, q + 1>(c, a0, a1, pf)
  P2(0); P2(2); P2(4); P2(6); P2(8); P2(10); P2(12); P2(14); P2(16); P2(18);
#pragma unroll
  for (int ks = 0; ks < 4; ++ks) c.aR[ks] = lds + 98304 + (wm * 64 + r) * 128 + ((tt ^ (2 * ks)) * 16);
  c.bR = lds + (8 * h + q4) * 256 + ((wn ^ q4) * 64) + ((lane >> 4) & 1) * 32 + (lane & 3) * 8;
  c.rI = mkrsrc(ka->w_in, DM * FI * 2); c.rK = mkrsrc(ws + WS_K, 32u << 20);
  c.soX = (zrb * 128 + wid * 16) * 1024; c.vo = lane * 16;
  c.soK = ((head * 64 + rb * 8 + wm * 2) * 8 + 2 * wn) * 1024;
  c.voF = F_XN + ((lane & 16 ? rb : 15 - rb) * 16 + (lane & 15)) * 8;
#pragma unroll
  for (int i = 0; i < 2; ++i) {
    const int o = (wid * 2 + i) * 64 + lane;
    c.voI[i] = wid < 6 && cg < 15 ? (o / 12) * (FI * 2) + (o % 12) * 16 + cg * 192 : 0x80000000u;
  }
  c.rP = mkrsrc(ka->wq, DM * 4096 * 2); c.voP = r * 64; c.voT = lane < 11 ? ((bid * 8 + wid) * 11 + lane) * 64 : 0x80000000u; c.soP = (bid * 8 + wid) * 2048;
  const int wr = wid & 3, kh = wid >> 2;
  K1ZA z; K1Z g0, g1;
#pragma unroll
  for (int k = 0; k < 2; ++k) z.xR[k] = lds + (wr * 32 + r) * 128 + ((tt ^ (2 * (2 * kh + k))) * 16);
  z.wR = lds + (32 * kh + 8 * h + q4) * 192 + ((lane >> 4) & 1) * 32 + (lane & 3) * 8;
  z.dR = z.wR + (wr % 3) * 64;
  unsigned long long ep;
  asm volatile("s_load_dwordx2 %0, %1, 0" : "=s"(ep) : "s"(ws + WS_F) : "memory");
  TS(1);
  WAITVL(NPRO); SB();
  asm volatile("" : "+s"(ep));
  fl.r = mkrsrc(ws + WS_F, 262144); fl.lo = (unsigned)(ep + 1); fl.hi = (unsigned)((ep + 1) >> 32); c.fl = fl;
  {
    asm volatile("" : "+v"(xv));
    float s = 0.f, q = 0.f;
#pragma unroll
    for (int e = 0; e < 4; ++e) { s = dot2(xv[e], 0x3f803f80u, s); q = dot2(xv[e], xv[e], q); }
#pragma unroll
    for (int o = 1; o < 32; o <<= 1) { s += __shfl_xor(s, o); q += __shfl_xor(q, o); }
    s = swap32_add(s); q = swap32_add(q);
    float mb, rs; ln_stats(s, q, 1.f / DM, mb, rs);
#pragma unroll
    for (int e = 0; e < 4; ++e) c.xn[e] = cvtpk((bflo(xv[e]) - mb) * rs, (bfhi(xv[e]) - mb) * rs);
    *(LAS u32x4*)(lds + 147456 + lane * 128 + wid * 16) = c.xn;
    static_assert(k1_seq(18).kind == 2 && k1_seq(18).item == 3 && k1_cum(-1) <= 18, "slot 9");
    c.rN = mkrsrc(ws + WS_XN, SEQ * DM * 2); c.voN = (lane >> 4) * 1024 + ((lane >> 3) & 1) * 512 + (lane & 7) * 16; c.soN = (bid >> 2) * 32768 + wid * 4096 + (bid & 3) * 128;
  }
  K1F f0, f1;
  SB(); WAITVL(k1_wait(0, 0)); BAR(); SB();
  TS(2);
  c.xn = *(LAS const u32x4*)(lds + 147456 + wid * 1024 + lane * 16);
  k1_rd<0, 0>(c, f0); k1_rd<0, 1>(c, f0); k1_rd<0, 2>(c, f0); k1_rd<0, 3>(c, f0); k1_rd<0, 4>(c, f0); k1_rd<0, 5>(c, f0); SB();
  k1_run<0, 8>(c, f0, f1, a0, a1, pf, z, g0);
  TS(3);
  k1_run<8, 16>(c, f0, f1, a0, a1, pf, z, g0);
  TS(4);
  k1_run<16, 24>(c, f0, f1, a0, a1, pf, z, g0);
  TS(5);
  k1_run<24, 32>(c, f0, f1, a0, a1, pf, z, g0);
  TS(6);
  z.acc[0] = (f32x16){}; z.acc[1] = (f32x16){}; z.acc[2] = (f32x16){}; z.s = 0.f; z.q = 0.f; z.cs = 0.f;
  k1z_stage<0>(c, a1, z, g0, g1, pf); k1z_stage<1>(c, a1, z, g1, g0, pf); k1z_stage<2>(c, a1, z, g0, g1, pf); k1z_stage<3>(c, a1, z, g1, g0, pf);
  k1z_stage<4>(c, a1, z, g0, g1, pf); k1z_stage<5>(c, a1, z, g1, g0, pf); k1z_stage<6>(c, a1, z, g0, g1, pf); k1z_stage<7>(c, a1, z, g1, g0, pf);
  TS(7);
  {
    LAS f32x4* ex = (LAS f32x4*)lds;
    LAS f32x2* st = (LAS f32x2*)(lds + 49152);
    LAS float* cs = (LAS float*)(lds + 51200);
    if (kh) k1z_send<1>(ex + wid * 384 + lane, z.acc, ze.own); else k1z_send<0>(ex + wid * 384 + lane, z.acc, ze.own);
    { const float s = swap32_add(z.s), q = swap32_add(z.q), cc = swap32_add(z.cs); st[wid * 32 + r] = (f32x2){s, q}; cs[wid * 32 + r] = cc; }
    WAITL0(); BAR(); SB();
    q.rA = mkrsrc(ws + WS_XN, SEQ * DM * 2); q.rB = c.rP;
    q.vo = c.vo; q.voB[0] = c.voB; q.voB[1] = c.voB + 31744;
    q.soA = ((kh ? rb : 15 - rb) * 4 + wr) * 32768; q.soB = wid * 65536 + head * 256; q.m0 = ldsb + 65536 + wid * 2048;
    gc_dma<8, 8192, 0, 0>(q); gc_dma<8, 8192, 0, 1>(q); gc_lda<8, 0, 0>(q, qr); gc_lda<8, 0, 1>(q, qr); gc_lda<8, 0, 2>(q, qr); gc_lda<8, 0, 3>(q, qr);
    gc_dma<8, 8192, 1, 0>(q); gc_dma<8, 8192, 1, 1>(q); gc_lda<8, 1, 0>(q, qr); gc_lda<8, 1, 1>(q, qr); gc_lda<8, 1, 2>(q, qr); gc_lda<8, 1, 3>(q, qr);
    SB();
    const f32x2 s0 = st[wr * 32 + r], s1 = st[(wr + 4) * 32 + r];
    float mb; ln_stats(s0[0] + s1[0], s0[1] + s1[1], 1.f / DM, mb, ze.rs);
    ze.mrs = -mb * ze.rs; ze.rz = mkrsrc(ws + WS_Z1, 2 * SEQ * FH * 2);
    ze.ex = ex + (wid ^ 4) * 384 + lane; ze.cs = cs + 16 * kh + 4 * h; ze.vo = c.vo; ze.k16 = 6 * cg + kh; ze.rb32 = zrb * 4 + wr;
    ze.gelu<0>();
    SB();
    gc_dma<8, 8192, 2, 0>(q); gc_dma<8, 8192, 2, 1>(q); gc_dma<8, 8192, 3, 0>(q); gc_dma<8, 8192, 3, 1>(q);
  }
  TS(8);
  asm volatile("" ::"v"(pf.t));
  TSW(1);
}

constexpr int TCH = 2;
constexpr float THR = 32.f;
struct K2C {
  rsrc_t rK, rV, rC;
  unsigned vo, ldsb;
  unsigned mS, mP;
  int n1, g, qrow;
  int rowA, rowB;
};
DI int k2_tile(const K2C& c, int n) { n = n > 16 ? 16 : n; return n < c.n1 ? n : 2 * n - c.n1 + c.g; }
DI unsigned k2_so(const K2C& c, int n) { return (n < c.n1 ? c.mS : (c.mP & 0xffffu)) + (unsigned)k2_tile(c, n) * 16384u; }
DI unsigned k2_m0(const K2C& c, int n) { return c.ldsb + (n < c.n1 ? c.mS : c.mP); }
DI LAS const char* k2_rd(const K2C& c, int n) { return (LAS const char*)(size_t)(c.vo + (c.ldsb + (n < c.n1 ? 0u : (c.mP & 0x10000u)))); }
template <int IMM> DI void dma16c(rsrc_t r, unsigned voff, unsigned soff, int a, int b) {
  asm volatile("s_cmp_lt_i32 %4, %5\n\ts_cbranch_scc1 .Ld%=\n\tbuffer_load_dwordx4 %0, %1, %2 offen offset:%3 lds\n.Ld%=:" ::"v"(voff), "s"(r), "s"(soff), "i"(IMM), "s"(a), "s"(b) : "scc", "memory");
}
DI void k2_mask(f32x16 (&s)[2], int dq) {
#pragma unroll
  for (int kb = 0; kb < 2; ++kb)
#pragma unroll
    for (int e = 0; e < 16; ++e) { const int cc = 32 * kb + (e & 3) + 8 * (e >> 2); if (cc > dq) s[kb][e] = -__builtin_inff(); }
}
DI float max3(float a, float b, float c) { float d; asm("v_max3_f32 %0, %1, %2, %3" : "=v"(d) : "v"(a), "v"(b), "v"(c)); return d; }
DI float k2_max(const f32x16 (&s)[2]) {
  float m0 = fmaxf(s[0][0], s[0][1]), m1 = fmaxf(s[1][0], s[1][1]);
#pragma unroll
  for (int e = 2; e < 16; e += 2) { m0 = fmaxf(fmaxf(m0, s[0][e]), s[0][e + 1]); m1 = fmaxf(fmaxf(m1, s[1][e]), s[1][e + 1]); }
  return fmaxf(m0, m1);
}
DI void addf(float& a, float b) { asm("v_add_f32 %0, %0, %1" : "+v"(a) : "v"(b)); }
DI void k2_loadq(u32x4 (&q)[8], unsigned la, int a, int b) {
  asm volatile("s_cmp_lg_u32 %9, %10\n\ts_cbranch_scc1 .Lq%=\n\t"
               "ds_read_b128 %0, %8\n\tds_read_b128 %1, %8 offset:1024\n\tds_read_b128 %2, %8 offset:2048\n\tds_read_b128 %3, %8 offset:3072\n\t"
               "ds_read_b128 %4, %8 offset:4096\n\tds_read_b128 %5, %8 offset:5120\n\tds_read_b128 %6, %8 offset:6144\n\tds_read_b128 %7, %8 offset:7168\n"
               ".Lq%=:" : "+v"(q[0]), "+v"(q[1]), "+v"(q[2]), "+v"(q[3]), "+v"(q[4]), "+v"(q[5]), "+v"(q[6]), "+v"(q[7])
               : "v"(la), "s"(a), "s"(b) : "scc", "memory");
}
struct K2S { f32x16 o[4], negm; u32x4 q[8]; bf16x8 f[3]; LAS const char* rk; float m, l; };

DI void k2_rescale(K2S& S, f32x16 (&sn)[2], float lmax, bool first) {
  float dl = swap32_max(lmax);
  if (first) { dl = dl == -__builtin_inff() ? 0.f : dl; S.m = dl; }
  else {
    dl = fmaxf(dl, 0.f); S.m += dl;
    const float f = __builtin_amdgcn_exp2f(-dl);
    S.l *= f;
#pragma unroll
    for (int db = 0; db < 4; ++db) S.o[db] *= f;
  }
#pragma unroll
  for (int kb = 0; kb < 2; ++kb) sn[kb] -= dl;
#pragma unroll
  for (int e = 0; e < 16; ++e) S.negm[e] = -S.m;
}
DI void k2_finishB(const K2C& c, K2S& S, unsigned ctx) {
  const float inv = __builtin_amdgcn_rcpf(swap32_add(S.l));
#pragma unroll
  for (int db = 0; db < 4; ++db) {
    const f32x16 res = S.o[db] * inv;
    bst16(pack8<0>(res), c.rC, c.vo, ctx + db * 2048); bst16(pack8<1>(res), c.rC, c.vo, ctx + db * 2048 + 1024);
    S.o[db] = (f32x16){};
  }
  S.l = 0.f;
}
DI void k2_finishA(const K2C& c, K2S& S, LAS char* lds, int wid, unsigned ctx) {
  const int lane = c.vo >> 4;
  const int g = wid >> 2, pw = wid ^ 4;
  LAS f32x4* ex = (LAS f32x4*)lds;
  LAS f32x2* es = (LAS f32x2*)(lds + 65536);
  const float lt = swap32_add(S.l);
  const float me = lt > 0.f ? S.m : -1e30f;
  es[wid * 64 + lane] = (f32x2){me, lt};
#pragma unroll
  for (int rd = 0; rd < 2; ++rd) {
    const f32x16 snd = g ? S.o[rd] : S.o[2 + rd];
#pragma unroll
    for (int e = 0; e < 4; ++e) ex[(wid * 8 + rd * 4 + e) * 64 + lane] = (f32x4){snd[4 * e], snd[4 * e + 1], snd[4 * e + 2], snd[4 * e + 3]};
  }
  WAITL0(); BAR();
  const f32x2 ps = es[pw * 64 + lane];
  const float mm = fmaxf(me, ps[0]);
  float w0 = __builtin_amdgcn_exp2f(me - mm), w1 = __builtin_amdgcn_exp2f(ps[0] - mm);
  const float inv = __builtin_amdgcn_rcpf(w0 * lt + w1 * ps[1]);
  w0 *= inv; w1 *= inv;
#pragma unroll
  for (int rd = 0; rd < 2; ++rd) {
    const f32x16 own = g ? S.o[2 + rd] : S.o[rd];
    f32x16 res;
#pragma unroll
    for (int e = 0; e < 4; ++e) {
      const f32x4 v = ex[(pw * 8 + rd * 4 + e) * 64 + lane];
#pragma unroll
      for (int c = 0; c < 4; ++c) res[4 * e + c] = own[4 * e + c] * w0 + v[c] * w1;
    }
    const unsigned p = ctx + (2 * g + rd) * 2048;
    bst16(pack8<0>(res), c.rC, c.vo, p); bst16(pack8<1>(res), c.rC, c.vo, p + 1024);
  }
}
template <int PAR> DI bf16x8 k2_frag(LAS const char* rd, int i) {
  if (i < 16) return ldsr(rd + (PAR ^ 1) * 16384 + i * 1024);
  if (i >= 32) return ldsr(rd + PAR * 16384 + (i - 32) * 1024);
  const int ks = (i - 16) >> 2, db = i & 3;
  return ldsr(rd + 32768 + PAR * 16384 + (ks >> 1) * 8192 + db * 2048 + (ks & 1) * 1024);
}
template <int PAR, bool NEXT> DI void k2_step(const K2C& c, K2S& S, int n, f32x16 (&sc)[2], f32x16 (&sn)[2], unsigned qnext, unsigned ctxB) {
  bf16x8 F[35];
  F[0] = S.f[0]; F[1] = S.f[1]; F[2] = S.f[2];
  u32x4 p[4];
  float l0 = S.l, l1 = 0.f, lmax = 0.f;
  unsigned soK = 0, soV = 0, soK2 = 0;
  LAS const char* rv = nullptr;
  if constexpr (NEXT) { soV = k2_so(c, n + 1); soK = k2_so(c, n + 3); soK2 = k2_so(c, n + 2); }
#pragma unroll
  for (int s = 0; s < 32; ++s) {
    if (s == 29) { asm volatile("s_waitcnt vmcnt(0) lgkmcnt(0)" ::: "memory"); BAR(); SB(); }
    if (s < 16) { if constexpr (NEXT) sn[s >> 3] = MFMA(F[s], __builtin_bit_cast(bf16x8, S.q[s & 7]), (s & 7) == 0 ? S.negm : sn[s >> 3]); }
    else S.o[s & 3] = MFMA(F[s], __builtin_bit_cast(bf16x8, p[(s - 16) >> 2]), S.o[s & 3]);
    SB();
    if (s == 0 && NEXT) F[3] = k2_frag<PAR>(S.rk, 3);
    if (s <= 27 && (NEXT || s >= 12)) F[s + 4] = k2_frag<PAR>(s < 12 ? S.rk : rv, s + 4);
    if (s >= 29 && NEXT) F[s + 3] = k2_frag<PAR>(S.rk, s + 3);
    if (s == 9) rv = k2_rd(c, n);
    if (s == 18 && NEXT) S.rk = k2_rd(c, n + 2);
    const int e0 = s < 5 ? 2 * s : s + 5, e1 = s < 5 ? 2 * s + 2 : s + 6;
    const int a0 = s < 6 ? 2 * s - 2 : s + 4, a1 = s < 6 ? 2 * s : s + 5;
    if (s <= 26) {
#pragma unroll
      for (int e = e0; e < e1; ++e) sc[e >> 4][e & 15] = __builtin_amdgcn_exp2f(sc[e >> 4][e & 15]);
    }
    if (s >= 1 && s <= 27) {
#pragma unroll
      for (int e = a0; e < a1; ++e) {
        if (e & 1) { addf(l1, sc[e >> 4][e & 15]); p[e >> 3][(e >> 1) & 3] = cvtpk(sc[e >> 4][(e & 15) - 1], sc[e >> 4][e & 15]); }
        else addf(l0, sc[e >> 4][e & 15]);
      }
    }
    if constexpr (NEXT) {
      if (s == 10) lmax = max3(sn[0][0], sn[0][1], sn[0][1]);
      if (s > 10 && s <= 17) lmax = max3(lmax, sn[0][2 * (s - 10)], sn[0][2 * (s - 10) + 1]);
      if (s >= 19 && s <= 26) lmax = max3(lmax, sn[1][2 * (s - 19)], sn[1][2 * (s - 19) + 1]);
      if (s == 1) dma16c<2048>(c.rK, c.vo, soK2, n + 2, c.n1);
      if (s == 3) dma16c<3072>(c.rK, c.vo, soK2, n + 2, c.n1);
      if (s == 5) dma16<0>(c.rV, c.vo, soV, k2_m0(c, n + 1) + 32768 + (PAR ^ 1) * 16384);
      if (s == 7) dma16n<1024>(c.rV, c.vo, soV);
      if (s == 9) dma16c<2048>(c.rV, c.vo, soV, n + 1, c.n1);
      if (s == 11) dma16c<3072>(c.rV, c.vo, soV, n + 1, c.n1);
      if (s == 16) k2_loadq(S.q, qnext, n + 2, c.g ? c.n1 : -1);
      if (s == 29) { dma16<0>(c.rK, c.vo, soK, k2_m0(c, n + 3) + (PAR ^ 1) * 16384); }
      if (s == 31) { dma16n<1024>(c.rK, c.vo, soK); }
    }
    if (s == 28) { S.l = l0 + l1; asm volatile("" : "+v"(S.l), "+v"(lmax)); }
    SB();
  }
  if constexpr (NEXT) {
    S.f[0] = F[32]; S.f[1] = F[33]; S.f[2] = F[34];
    const bool first = c.g && n + 1 == c.n1;
    const int kb = k2_tile(c, n + 1) * 64, rw = n + 1 < c.n1 ? c.rowB : c.rowA;
    if (kb + 63 > rw) { k2_mask(sn, rw + c.qrow - kb); lmax = k2_max(sn); }
    if (first || __any(lmax > THR)) k2_rescale(S, sn, lmax, first);
    if (first) k2_finishB(c, S, ctxB);
    if (c.g && n + 2 == c.n1) {
#pragma unroll
      for (int e = 0; e < 16; ++e) S.negm[e] = 0.f;
    }
    SB();
  }
}
DI void ld16(u32x4& v, rsrc_t r, unsigned vo, unsigned so) { asm volatile("buffer_load_dwordx4 %0, %1, %2, %3 offen sc1" : "=v"(v) : "v"(vo), "s"(r), "s"(so) : "memory"); }
struct QX {
  const FL& fl; const K2C& c; const ZE& ze; rsrc_t rz; unsigned voF, voZ, zo[2]; LAS char* kw; int bid, wid; u32x2 fk; u32x4 zv[2], kv[6];
  static constexpr int extra(int t) { return t == 1 ? 2 : t == 0 || t == 3 ? 6 : t == 5 ? 1 : 0; }
  template <int N> DI void at() {
    if constexpr (N == 4) fl_ld(fl, fk, voF);
    if constexpr (N < 8 && !(N & 1)) { if (c.g == ((N >> 1) & 1)) ze.gelu<1 + N / 4>(); }
    if constexpr (N == 11) fl_pub(fl, F_Z + bid * 8, wid == 0 && c.vo == 0);
    if constexpr (N == 20) fl_ld(fl, fk, voZ);
    if constexpr (N == 27) {
      fl_chk<false>(fl, fk, voZ); ld16(zv[0], rz, zo[0], 0); ld16(zv[1], rz, zo[1], 0);
      if (wid == 0) asm volatile("buffer_inv sc1" ::: "memory");
    }
    if constexpr (N >= 12 && N < 15) { ld16(kv[2 * N - 24], N == 14 ? c.rV : c.rK, c.vo, c.mS + (N == 13) * 16384); ld16(kv[2 * N - 23], N == 14 ? c.rV : c.rK, c.vo, c.mS + (N == 13) * 16384 + 1024); }
    if constexpr (N >= 20 && N < 23) {
      asm volatile("" : "+v"(kv[2 * N - 40]), "+v"(kv[2 * N - 39]));
      *(LAS u32x4*)(kw + (N - 20) * 16384) = kv[2 * N - 40]; *(LAS u32x4*)(kw + (N - 20) * 16384 + 1024) = kv[2 * N - 39];
    }
  }
  template <int T> DI void pre() { if constexpr (T == 2) { if (wid == 0) { asm volatile("s_waitcnt vmcnt(10)" : "+v"(fk)::"memory"); fl_chk<false>(fl, fk, voF); } } }
};
template <int N> DI void ln_part(const u32x4 (&v)[N], LAS f32x2* st, int wid, int lane) {
  float s = 0.f, q = 0.f;
#pragma unroll
  for (int i = 0; i < N; ++i)
#pragma unroll
    for (int e = 0; e < 4; ++e) { const float a = bflo(v[i][e]), b = bfhi(v[i][e]); s += a + b; q = fmaf(a, a, fmaf(b, b, q)); }
  s += __shfl_xor(s, 8); q += __shfl_xor(q, 8); s += __shfl_xor(s, 16); q += __shfl_xor(q, 16);
  s = swap32_add(s); q = swap32_add(q);
  if (lane < 8) st[lane * 8 + wid] = (f32x2){s, q};
}
template <int N> DI void ln_apply(u32x4 (&v)[N], LAS const f32x2* st, int lane, float invn) {
  float s = 0.f, q = 0.f;
#pragma unroll
  for (int i = 0; i < 4; ++i) { const f32x4 t = *(LAS const f32x4*)(st + (lane & 7) * 8 + 2 * i); s += t[0] + t[2]; q += t[1] + t[3]; }
  float mb, rs; ln_stats(s, q, invn, mb, rs);
#pragma unroll
  for (int i = 0; i < N; ++i)
#pragma unroll
    for (int e = 0; e < 4; ++e) v[i][e] = cvtpk((bflo(v[i][e]) - mb) * rs, (bfhi(v[i][e]) - mb) * rs);
}

constexpr int k3_cum(int t) { const int a = 2 * t + 4, b = t + 6; return a < b ? a : b < 32 ? b : 32; }
struct WO {
  rsrc_t rw; unsigned vo, vl, l0, so;
  DI void dma(int pc) const { dma16<0>(rw, pc >= 9 ? vl : vo, so + pc * 8192, l0 + pc * 1024); }
  FL fl; u32x2 f; unsigned voF; int wid;
  static constexpr int extra(int t) { return (t >= 2 && t < 13) + (t == 3); }
  template <int N> DI void at() { if constexpr (N % 4 == 1 && N / 4 >= 2 && N / 4 < 13) dma(N / 4 - 2); if constexpr (N == 12) fl_ld(fl, f, voF); }
  template <int T> DI void pre() { if constexpr (T == 5) { if (wid == 0) fl_chk<false>(fl, f, voF); } }
};
struct K3B { rsrc_t rA, rB; unsigned voA[2], voB[2], soA, m0; LAS const char* aR; LAS const char* bR; };
struct K3F { bf16x8 a[2], w[3]; };
template <int T, int P> DI void k3b_dma(const K3B& c) {
  if constexpr (T < 32) {
    if constexpr (P == 0) dma16<0>(c.rA, c.voA[0], c.soA + T * 128, c.m0 + ((T + 3) % 5) * 32768);
    if constexpr (P == 1) dma16n<1024>(c.rA, c.voA[1], c.soA + T * 128);
    if constexpr (P == 2) dma16<0>(c.rB, c.voB[0], T * (64 * ZLD * 2), c.m0 + ((T + 3) % 5) * 32768 + 16384);
    if constexpr (P == 3) dma16n<1024>(c.rB, c.voB[1], T * (64 * ZLD * 2));
  }
}
template <int T, int G> DI void k3b_rd(const K3B& c, K3F& f) {
  if constexpr (T < 32) {
    constexpr int o = ((T + 3) % 5) * 32768;
    if constexpr (G == 1) { f.a[0] = ldsr(c.aR + o); f.a[1] = ldsr(c.aR + o + 4096); }
    else { constexpr int j = G ? G - 1 : 0; f.w[j] = ldstr(c.bR + o + j * 64, c.bR + o + j * 64 + 8 * 192); }
  }
}
template <int T> DI void k3b_stage(const K3B& c, const K3F& f, K3F& fn, f32x16 (&o)[6]) {
  constexpr int N = k3_cum(T - 1), M = k3_cum(T) - N == 2 ? N + 1 : 32;
  if constexpr (T < 31) { WAITVL(4 * (N - T - 2) + (T == 0 ? 4 : 0)); BAR(); SB(); }
#define BM(i, j) o[3 * (i) + (j)] = MFMA(f.w[j], f.a[i], o[3 * (i) + (j)]); SB()
  BM(0, 0); k3b_rd<T + 1, 0>(c, fn); SB(); BM(1, 0); k3b_rd<T + 1, 1>(c, fn); k3b_dma<N, 0>(c); SB(); BM(0, 1); k3b_rd<T + 1, 2>(c, fn); k3b_dma<N, 1>(c); SB();
  BM(1, 1); k3b_rd<T + 1, 3>(c, fn); k3b_dma<N, 2>(c); SB(); BM(0, 2); k3b_dma<N, 3>(c); k3b_dma<M, 0>(c); k3b_dma<M, 1>(c); SB(); BM(1, 2); k3b_dma<M, 2>(c); k3b_dma<M, 3>(c); SB();
}
template <int T> DI void k3b_run(const K3B& c, K3F& f0, K3F& f1, f32x16 (&o)[6]) {
  if constexpr (T < 32) { k3b_stage<T>(c, f0, f1, o); k3b_stage<T + 1>(c, f1, f0, o); k3b_run<T + 2>(c, f0, f1, o); }
}
template <int S> DI void unit_put(LAS char* p, const f32x16& a) {
  *(LAS f32x4*)p = (f32x4){a[8 * S], a[8 * S + 1], a[8 * S + 2], a[8 * S + 3]}; *(LAS f32x4*)(p + 1024) = (f32x4){a[8 * S + 4], a[8 * S + 5], a[8 * S + 6], a[8 * S + 7]};
}
template <int KG, int J> DI void unit_send(LAS char* ex, const f32x16 (&o)[2 * J], float (&v)[J][8]) {
#pragma unroll
  for (int u = 0; u < 4; ++u)
#pragma unroll
    for (int j = 0; j < J; ++j) {
      if (u == KG) {
#pragma unroll
        for (int e = 0; e < 8; ++e) { v[j][e] = o[J * (u >> 1) + j][8 * (u & 1) + e]; asm volatile("" : "+v"(v[j][e])); }
      } else {
        LAS char* p = ex + ((u * 3 + (KG > u ? KG - 1 : KG)) * J + j) * 2048;
        if (u & 1) unit_put<1>(p, o[J * (u >> 1) + j]); else unit_put<0>(p, o[J * (u >> 1) + j]);
      }
    }
}
template <int J> DI void unit_reduce(LAS char* ex, int kg, const f32x16 (&o)[2 * J], float (&v)[J][8]) {
  if (kg == 0) unit_send<0, J>(ex, o, v); else if (kg == 1) unit_send<1, J>(ex, o, v); else if (kg == 2) unit_send<2, J>(ex, o, v); else unit_send<3, J>(ex, o, v);
  WAITL0(); BAR(); SB();
  ex += kg * (3 * J * 2048);
#pragma unroll
  for (int j = 0; j < J; ++j)
#pragma unroll
    for (int s = 0; s < 3; ++s) {
      const f32x4 a = *(LAS const f32x4*)(ex + (s * J + j) * 2048), b = *(LAS const f32x4*)(ex + (s * J + j) * 2048 + 1024);
#pragma unroll
      for (int e = 0; e < 4; ++e) { v[j][e] += a[e]; v[j][4 + e] += b[e]; }
    }
}

__global__ __launch_bounds__(512, 2) void mega(Args) {
  extern __shared__ __attribute__((aligned(16))) char lds_[];
  LAS char* lds = (LAS char*)lds_;
  const unsigned ldsb = (unsigned)(size_t)lds;
  FL fl;
  GC q; GR qr; ZE ze;
  p1_kvz(lds, fl, q, qr, ze);

  int tid = threadIdx.x;
  asm volatile("" : "+v"(tid));
  char* ws = kargs()->ws;
  const int wid = rfl(tid >> 6);
  const int bid = blockIdx.x, xcd = bid & 7, idx = bid >> 3;
  TSD; TS(0);
  K2C c;
  K2S S;
  unsigned ctxA;
  {
    const int lane = tid & 63, r = lane & 31, h = lane >> 5, q4 = (lane >> 2) & 3;
    const int head = xcd * 4 + (idx >> 3), j = idx & 7, g = wid >> 2, wq = wid & 3;
    c.rK = mkrsrc(ws + WS_K + (size_t)head * 524288, 524288); c.rV = mkrsrc(ws + WS_V + (size_t)head * 524288, 524288);
    c.rC = mkrsrc(ws + WS_C, 16u << 20);
    c.vo = lane * 16; c.ldsb = ldsb; c.mS = wid * 2048; c.mP = g * 65536 + wq * 4096;
    c.n1 = 2 * j + 2; c.g = g; c.qrow = r - 4 * h;
    const int rbA = (15 - j) * 4 + wq, rbB = g ? j * 4 + wq : rbA;
    c.rowA = rbA * 32; c.rowB = rbB * 32;
    ctxA = (rbA * 32 + head) * 8192;
    const unsigned ctxB = (rbB * 32 + head) * 8192;
    const int ch = tid >> 3, zr = bid * 8 + (lane & 7), zcg = 7 + (lane & 7);
    QX x{fl, c, ze};
    x.bid = bid; x.wid = wid; x.rz = mkrsrc(ws + WS_Z2, SEQ * FH * 2);
    x.voF = F_KV + (((idx & ~7) + (lane & 7)) * 8 + xcd) * 8; x.voZ = F_Z + ((((bid >> 4) & 3) * 8 + (zcg & 7)) * 8 + (bid >> 6) * 2 + (zcg >> 3)) * 8;
    x.kw = lds + c.mS + lane * 16;
    x.zo[0] = (zr >> 5) * 43 * 1024 + (ch >> 1) * 1024 + (ch & 1) * 512 + (zr & 31) * 16; x.zo[1] = ch < 22 ? x.zo[0] + 32768 : 0x80000000u;
    LAS f32x2* st = (LAS f32x2*)(lds + 98304);
#pragma unroll
    for (int i = 0; i < 4; ++i) { q.bR[i] = lds + 65536 + (8 * h + q4) * 256 + ((i ^ q4) * 64) + ((lane >> 4) & 1) * 32 + (lane & 3) * 8; S.o[i] = (f32x16){}; }
    TS(1);
    WAITV(11); BAR(); SB();
    fl_pub(fl, F_KV + bid * 8, tid == 0);
    TS(2);
    gc_first<8, 1024>(q, qr);
    gc_run<8, 8192, 1024, 0>(q, qr, S.o, x);
    TS(3);
#pragma unroll
    for (int i = 0; i < 4; ++i) {
      S.o[i] *= QSCALE;
      S.q[2 * i] = pack8<0>(S.o[i]); S.q[2 * i + 1] = pack8<1>(S.o[i]);
      S.o[i] = (f32x16){};
    }
    if (!g) {
#pragma unroll
      for (int i = 0; i < 8; ++i) *(LAS u32x4*)(lds + 131072 + wq * 8192 + i * 1024 + lane * 16) = S.q[i];
    }
    S.negm = (f32x16){}; S.m = 0.f; S.l = 0.f;
    f32x16 sa[2], sb[2];
    const unsigned qA = c.ldsb + 131072 + wq * 8192 + c.vo;
    S.rk = lds + lane * 16;
#pragma unroll
    for (int s = 0; s < 16; ++s) sa[s >> 3] = MFMA(k2_frag<1>(S.rk, s), __builtin_bit_cast(bf16x8, S.q[s & 7]), (s & 7) == 0 ? S.negm : sa[s >> 3]);
    asm volatile("s_waitcnt vmcnt(0)" : "+v"(x.zv[0]), "+v"(x.zv[1])::"memory");
    ln_part<2>(x.zv, st, wid, lane);
    if (63 > c.rowB) k2_mask(sa, c.rowB + c.qrow);
    k2_rescale(S, sa, k2_max(sa), true);
    SB();
    WAITL0(); BAR(); SB();
    dma16<0>(c.rK, c.vo, k2_so(c, 2), k2_m0(c, 2)); dma16n<1024>(c.rK, c.vo, k2_so(c, 2));
#pragma unroll
    for (int i = 0; i < 3; ++i) S.f[i] = k2_frag<0>(S.rk, i);
    {
      ln_apply<2>(x.zv, st, lane, 1.f / FH);
      const rsrc_t ro = mkrsrc(ws + WS_Z2N, SEQ * ZLD * 2);
      const unsigned oo = ((zr & ~12) | ((zr & 4) << 1) | ((zr & 8) >> 1)) * (ZLD * 2) + ch * 16;
      if (ch >= 22) x.zv[1] = ((u32x4){0u, 0u, 0u, 0u});
      bst16(x.zv[0], ro, oo, 0); bst16(x.zv[1], ro, ch < 32 ? oo + 1024 : 0x80000000u, 0);
    }
    unsigned pf = 0;
    TS(4);
    for (int n = 0; n < 16; n += 2) {
      k2_step<0, true>(c, S, n, sa, sb, qA, ctxB);
      if (n == 0) fl_pub(fl, F_ZN + bid * 8, wid == 0 && c.vo == 0);
      if (n == 8 && xcd >= 4 && wid == 0) {
        u32x2 fz[4];
#pragma unroll
        for (int i = 0; i < 4; ++i) fl_ld(fl, fz[i], F_ZN + i * 512 + (c.vo >> 1));
        WAITV(0);
        fl_chk<false>(fl, fz[0], F_ZN + (c.vo >> 1)); fl_chk<false>(fl, fz[1], F_ZN + 512 + (c.vo >> 1));
        fl_chk<false>(fl, fz[2], F_ZN + 1024 + (c.vo >> 1)); fl_chk<true>(fl, fz[3], F_ZN + 1536 + (c.vo >> 1));
        SB();
      }
      if (n >= TCH && n < TCH + 6) {
        const KA ka = kargs();
        const unsigned w = bid * 8 + wid;
        const rsrc_t rp = n == TCH ? mkrsrc(ka->wsp, SEQ * SEQ * 2) : n == TCH + 2 ? mkrsrc(ka->wo, 4096 * DM * 2) : mkrsrc(ka->wout, FH * DM * 2);
        asm volatile("buffer_load_dword %0, %1, %2, %3 offen" : "+v"(pf) : "v"(c.vo * 4), "s"(rp), "s"(n == TCH + 2 ? (w & 1023) * 4096 : w < 2048 - (n - TCH) * 469 ? w * 4096 : 0) : "memory");
        SB();
      }
      k2_step<1, true>(c, S, n + 1, sb, sa, qA, ctxB);
    }
    asm volatile("" ::"v"(pf));
    TS(5);
    k2_step<0, false>(c, S, 16, sa, sb, qA, ctxB);
    TS(6);
  }
  GC ca; K3B cb;
  asm volatile("" : "+v"(c.vo));
  const int lane = c.vo >> 4, r = lane & 31, h = lane >> 5, q4 = (lane >> 2) & 3;
  const int rb3 = xcd < 4 ? idx >> 2 : (xcd - 4) * 4 + (idx >> 3), wm = wid & 1, kg = wid >> 1;
  const int k16 = 6 * (idx & 7) + (kg & 1);
  const unsigned zo = ((rb3 * 4 + 2 * wm + (kg >> 1)) * 43 + k16) * 1024;
  if (xcd < 4) {
    ca.rA = c.rC; ca.rB = mkrsrc(kargs()->wo, 4096 * DM * 2);
    ca.vo = lane * 16; ca.voB[0] = (lane >> 4) * 1024 + (((lane & 15) ^ ((lane >> 4) << 2)) * 16); ca.voB[1] = ca.voB[0] + 3072;
    ca.soA = ((rb3 * 8 + wid) * 256 + xcd * 64) * 1024; ca.soB = (xcd * 1024 + wid * 8) * 1024 + (idx & 3) * 256; ca.m0 = ldsb + 98304 + wid * 2048;
    gc_dma4<16, 1024>(ca);
  } else {
    const KA ka = kargs();
    cb.rA = mkrsrc(ka->wsp, SEQ * SEQ * 2); cb.rB = mkrsrc(ws + WS_Z2N, SEQ * ZLD * 2);
#pragma unroll
    for (int i = 0; i < 2; ++i) {
      cb.voA[i] = (8 * i + (lane >> 3)) * 4096 + (((lane & 7) ^ (4 * i + (lane >> 4))) * 16) - i * 1024;
      const int o = (2 * wid + i) * 64 + lane;
      cb.voB[i] = wid < 6 ? (o / 12) * (ZLD * 2) + (o % 12) * 16 + (idx & 7) * 192 - i * 1024 : 0x80000000u;
    }
    cb.soA = (rb3 * 128 + wid * 16) * 4096; cb.m0 = ldsb + wid * 2048;
#define B4(T) k3b_dma<T, 0>(cb); k3b_dma<T, 1>(cb); k3b_dma<T, 2>(cb); k3b_dma<T, 3>(cb)
    B4(0); B4(1);
  }
  SB();
  k2_finishA(c, S, lds, wid, ctxA);
  u32x2 fc;
  const int cblk = 2 * (idx >> 2) + ((lane >> 2) & 1);
  const unsigned voC = F_CTX + (((lane & 3) * 8 + (cblk < 8 ? cblk : 15 - cblk)) * 8 + 2 * (xcd & 3)) * 128;
  fl_ld(fl, fc, voC);
  TS(7); WAITV(0); BAR(); SB();
  fl_pub(fl, F_CTX + bid * 128, wid == 0 && lane == 0);
  TS(8); TSW(2); TSR; TS(0);
  WO wo;
  wo.rw = mkrsrc(kargs()->wout, FH * DM * 2); wo.vo = (lane >> 3) * 1024 + (((lane & 7) ^ (((lane >> 4) & 1) << 2)) * 16); wo.vl = wid == 7 ? 0x80000000u : wo.vo;
  wo.l0 = ldsb + wid * 11264; wo.so = wid * 11 * 8192 + (idx >> 2) * 128;
  if (xcd < 4) {
    GR g;
    if (wid == 0) fl_chk<false>(fl, fc, voC);
    BAR(); SB();
    TS(1);
    gc_lda2<16>(ca, g);
    wo.fl = fl; wo.wid = wid; wo.voF = voC + 128;
#pragma unroll
    for (int i = 0; i < 4; ++i) ca.bR[i] = lds + 98304 + (4 * h + q4) * 256 + ((i ^ q4) * 64) + ((lane >> 4) & 1) * 32 + (lane & 3) * 8;
    f32x16 o[4] = {};
    WAITV(4); SB();
    TS(2);
    gc_first<16, 2048>(ca, g);
    gc_run<16, 1024, 2048, 0>(ca, g, o, wo);
    TS(3);
    const rsrc_t rp = mkrsrc(ws + WS_P, 8u << 20);
    const unsigned so = ((xcd * 64 + rb3 * 8 + wid) * 16 + (idx & 3) * 4) * 2048;
#pragma unroll
    for (int i = 0; i < 4; ++i) { bst16(pack8<0>(o[i]), rp, ca.vo, so + i * 2048); bst16(pack8<1>(o[i]), rp, ca.vo, so + i * 2048 + 1024); }
  } else {
    u32x4 z[3]; float b;
    const rsrc_t rz = mkrsrc(ws + WS_Z1, SEQ * FH * 2);
#pragma unroll
    for (int j = 0; j < 3; ++j) asm volatile("buffer_load_dwordx4 %0, %1, %2, %3 offen sc1" : "=v"(z[j]) : "v"(lane * 16), "s"(rz), "s"(zo + j * 2048) : "memory");
    asm volatile("buffer_load_ushort %0, %1, %2, %3 offen" : "=v"(b) : "v"(r * 2), "s"(mkrsrc(kargs()->bsp, SEQ * 2)), "s"((rb3 * 4 + 2 * wm + (kg >> 1)) * 64) : "memory");
    cb.aR = lds + (wm * 64 + r) * 128 + ((h ^ ((r >> 1) & 7) ^ (2 * kg)) * 16);
    cb.bR = lds + 16384 + (16 * kg + 4 * h + q4) * 192 + ((lane >> 4) & 1) * 32 + (lane & 3) * 8;
    f32x16 o[6] = {};
    K3F f0, f1;
    k3b_rd<0, 0>(cb, f0); k3b_rd<0, 1>(cb, f0); k3b_rd<0, 2>(cb, f0); k3b_rd<0, 3>(cb, f0); SB();
    k3b_run<0>(cb, f0, f1, o);
    TS(3);
    BAR();
    asm volatile("" : "+v"(z[0]), "+v"(z[1]), "+v"(z[2]), "+v"(b));
    b = __uint_as_float(__float_as_uint(b) << 16);
    float v[3][8];
    unit_reduce<3>(lds + wm * 73728 + lane * 16, kg, o, v);
    const rsrc_t rh = mkrsrc(ws + WS_H, SEQ * FH * 2);
#pragma unroll
    for (int j = 0; j < 3; ++j) {
      u32x4 w;
#pragma unroll
      for (int e = 0; e < 4; ++e) w[e] = cvtpk(bflo(z[j][e]) * (v[j][2 * e] + b), bfhi(z[j][e]) * (v[j][2 * e + 1] + b));
      if (k16 + 2 * j < 43) bst16(w, rh, lane * 16, zo + j * 2048);
    }
    fl_pub(fl, 0, bid == 7 && wid == 0 && lane == 0);
  }
  {
    const int rb = xcd * 4 + (idx & 3), cb = idx >> 2, rb32 = rb * 2 + wm;
    u32x4 hf[11], pp[4]; u32x2 xv[2], fh;
    const unsigned voH = F_P + ((((rb >> 1) & 3) * 8 + (lane & 7)) * 8 + 4 + (rb >> 3)) * 128, voP = F_P + ((xcd * 4 + (cb >> 1)) * 8 + (lane & 3)) * 128;
    fl_ld(fl, fh, voH);
    TS(4); WAITV(0); BAR(); SB();
    fl_pub(fl, F_P + bid * 128, wid == 0 && lane == 0);
    TS(5); TSW(3); TSR; TS(0);
    const KA ka = kargs();
    if (xcd >= 4) {
#pragma unroll
      for (int pc = 0; pc < 11; ++pc) wo.dma(pc);
    }
    if (wid == 7) fl_chk<false>(fl, fh, voH);
    BAR(); SB();
    TS(1);
    {
      const rsrc_t rh = mkrsrc(ws + WS_H, SEQ * FH * 2), rx = mkrsrc(ka->x, SEQ * DM * 2);
#pragma unroll
      for (int i = 0; i < 11; ++i) ld16(hf[i], rh, i == 10 && kg == 3 ? 0x80000000u : lane * 16, (rb32 * 43 + kg * 11 + i) * 1024);
      fl_ld(fl, fh, voP);
#pragma unroll
      for (int i = 0; i < 2; ++i)
        asm volatile("buffer_load_dwordx2 %0, %1, %2, %3 offen offset:%4" : "=v"(xv[i]) : "v"(r * 1024 + h * 8), "s"(rx), "s"(rb32 * 32768 + cb * 128 + kg * 32), "i"(i * 16) : "memory");
    }
    LAS const char* bR[2];
#pragma unroll
    for (int j = 0; j < 2; ++j) bR[j] = lds + kg * 22528 + (4 * h + q4) * 128 + ((j ^ (q4 >> 1)) * 64) + ((lane >> 4) & 1) * 32 + (lane & 3) * 8;
    f32x16 o[2] = {};
    WAITV(3); BAR(); SB();
    TS(2);
#pragma unroll
    for (int i = 0; i < 11; ++i) asm volatile("" : "+v"(hf[i]));
#pragma unroll
    for (int ks = 0; ks < 11; ++ks)
#pragma unroll
      for (int j = 0; j < 2; ++j) o[j] = MFMA(ldstr(bR[j] + ks * 2048, bR[j] + ks * 2048 + 1024), __builtin_bit_cast(bf16x8, hf[ks]), o[j]);
    TS(3);
    WAITVL(0);
    if (wid == 7) fl_chk<false>(fl, fh, voP);
    BAR(); SB();
    const rsrc_t rp = mkrsrc(ws + WS_P, 8u << 20);
#pragma unroll
    for (int i = 0; i < 4; ++i) ld16(pp[i], rp, lane * 16, ((i * 64 + rb32) * 32 + cb * 4 + kg) * 1024);
    float v[1][8];
    unit_reduce<1>(lds + wm * 24576 + lane * 16, kg, o, v);
    asm volatile("s_waitcnt vmcnt(0)" : "+v"(pp[0]), "+v"(pp[1]), "+v"(pp[2]), "+v"(pp[3]), "+v"(xv[0]), "+v"(xv[1])::"memory");
    u32x4 w;
#pragma unroll
    for (int e = 0; e < 4; ++e)
      w[e] = cvtpk(v[0][2 * e] + bflo(xv[e >> 1][e & 1]) + ((bflo(pp[0][e]) + bflo(pp[1][e])) + (bflo(pp[2][e]) + bflo(pp[3][e]))),
                   v[0][2 * e + 1] + bfhi(xv[e >> 1][e & 1]) + ((bfhi(pp[0][e]) + bfhi(pp[1][e])) + (bfhi(pp[2][e]) + bfhi(pp[3][e]))));
    bst16<0>(natural8(w), mkrsrc(ka->out, SEQ * DM * 2), r * 1024 + h * 16, rb32 * 32768 + cb * 128 + kg * 32);
    TS(4); TSW(4);
  }
}

extern "C" void kernel_launch(void* const* d_in, const int* in_sizes, int n_in, void* d_out, int out_size, void* d_ws, size_t ws_size, hipStream_t stream) {
  (void)in_sizes; (void)n_in; (void)out_size; (void)ws_size;
  const Args a = {(const u16*)d_in[0], (const u16*)d_in[1], (const u16*)d_in[2], (const u16*)d_in[3], (const u16*)d_in[4], (const u16*)d_in[5],
                  (const u16*)d_in[7], (const u16*)d_in[8], (const u16*)d_in[9], (const u16*)d_in[10], (char*)d_ws, (u16*)d_out};
  (void)hipFuncSetAttribute((const void*)mega, hipFuncAttributeMaxDynamicSharedMemorySize, LDSZ);
  mega<<<256, 512, LDSZ, stream>>>(a);
}
```
